```python
import torch

def _softmax(x, axis=-1):
    return torch.softmax(x, dim=axis)
def _gelu(x):
    return torch.nn.functional.gelu(x, approximate="tanh")
def _silu(x):
    return torch.nn.functional.silu(x)
def _sigmoid(x):
    return torch.sigmoid(x)
def _layernorm(x, axis=-1, eps=1e-5):
    m = x.mean(dim=axis, keepdim=True)
    v = x.var(dim=axis, keepdim=True, unbiased=False)
    return (x - m) / torch.sqrt(v + eps)
def _scatter_add(tgt, idx, src, axis=0):
    return tgt.clone().index_add_(axis, idx, src)
def _segment_sum(src, seg, n):
    out = torch.zeros((n,) + src.shape[1:], dtype=src.dtype)
    return out.index_add_(0, seg, src)
def _pad(x, pw):
    flat = tuple(v for pair in reversed(pw) for v in pair)
    return torch.nn.functional.pad(x, flat)

kernel_name = 'gated_mix__sliding_block__mixer_s_mlp__prompt'

def setup_inputs():

    _w = (lambda a: (lambda t: t.to(torch.bfloat16) if t.is_floating_point() else t)(torch.as_tensor(a)))
    def _A_setup():
        B, S, D = 1, 2048, 448
        return {
            "x": _w((lambda *s: torch.randn(*s, dtype=torch.bfloat16) * 0.05)(B, S, D)),
            "wqkv": _w((lambda *s: torch.randn(*s, dtype=torch.bfloat16) * 3.2)(D, 3 * D)),
            "wo": _w((lambda *s: torch.randn(*s, dtype=torch.bfloat16) * 0.05)(D, D)),
        }
    _A_in = _A_setup()

    def _B_setup():
        B, S, D, F = 1, 2048, 448, 1792
        return {
            "x":  _w((lambda *s: torch.randn(*s, dtype=torch.bfloat16) * 0.05)(B, S, D)),
            "w1": _w((lambda *s: torch.randn(*s, dtype=torch.bfloat16) * 3.2)(D, D) * (D ** -0.5)),
            "w2": _w((lambda *s: torch.randn(*s, dtype=torch.bfloat16) * 3.2)(D, D) * (D ** -0.5)),
            "w3": _w((lambda *s: torch.randn(*s, dtype=torch.bfloat16) * 3.2)(D, F) * (D ** -0.5)),
            "w4": _w((lambda *s: torch.randn(*s, dtype=torch.bfloat16) * 3.2)(F, D) * (F ** -0.5)),
        }
    _B_in = _B_setup()

    return {
        "A_x": _A_in["x"],
        "A_wqkv": _A_in["wqkv"],
        "A_wo": _A_in["wo"],
        "B_x": _B_in["x"],
        "B_w1": _B_in["w1"],
        "B_w2": _B_in["w2"],
        "B_w3": _B_in["w3"],
        "B_w4": _B_in["w4"],
        "gate_w": _w((lambda *s: torch.randn(*s, dtype=torch.bfloat16) * 0.05)(448) * 16.0),
    }

def reference(A_x, A_wqkv, A_wo, B_x, B_w1, B_w2, B_w3, B_w4, gate_w):
    def _A_ref(x, wqkv, wo):
        B, S, D = x.shape
        H = 16; Dh = D // H; W = 256
        qkv = (lambda x, *d: x.permute(*d))((x @ wqkv).reshape(B, S, 3, H, Dh), 2, 0, 3, 1, 4)
        q, k, v = qkv[0], qkv[1], qkv[2]
        scores = torch.einsum("bhqd,bhkd->bhqk", q, k) * (Dh ** -0.5)
        idx = torch.arange(S)
        mask = (idx[:, None] - idx[None, :] >= 0) & (idx[:, None] - idx[None, :] < W)
        scores = torch.where(mask[None, None], scores, -1e9)
        p = _softmax(scores, axis=-1)
        o = torch.einsum("bhqk,bhkd->bhqd", p, v)
        return (lambda x, *d: x.permute(*d))(o, 0, 2, 1, 3).reshape(B, S, D) @ wo

    def _B_ref(x, w1, w2, w3, w4):
        B, S, D = x.shape
        h = _gelu(_layernorm(x, axis=-1) @ w1)
        c = D // 4
        g0 = _pad(h[:, :, 0*c:1*c], ((0, 0), (1, 0), (0, 0)))[:, :S, :]
        g1 = _pad(h[:, :, 1*c:2*c], ((0, 0), (0, 1), (0, 0)))[:, 1:, :]
        g2 = _pad(h[:, :, 2*c:3*c], ((0, 0), (2, 0), (0, 0)))[:, :S, :]
        g3 = _pad(h[:, :, 3*c:   ], ((0, 0), (0, 2), (0, 0)))[:, 2:, :]
        hs = torch.concatenate([g0, g1, g2, g3], axis=-1) @ w2
        x1 = x + hs
        h2 = _gelu(_layernorm(x1, axis=-1) @ w3) @ w4
        return x1 + h2

    x = A_x
    g = _sigmoid((x * gate_w).sum(-1, keepdims=True))
    a = _A_ref(x, A_wqkv, A_wo)
    b = _B_ref(x, B_w1, B_w2, B_w3, B_w4)
    return g * a + (1 - g) * b
```

```cpp
#include <hip/hip_runtime.h>
#include <hip/hip_bf16.h>
#include <stdint.h>

typedef __hip_bfloat16 bf16;
#define S_ 2048
#define D_ 448
#define F_ 1792
#define H_ 16
#define DH_ 28
#define W_ 256

__device__ inline float b2f(bf16 v){ return __bfloat162float(v);}
__device__ inline bf16 f2b(float v){ return __float2bfloat16(v);}
__device__ inline float gelu_f(float x){ float u = 0.7978845608028654f*(x+0.044715f*x*x*x); return 0.5f*x*(1.f+tanhf(u)); }

__global__ void gemm_naive(const bf16* A, const bf16* B, bf16* C, int M, int N, int K, int mode){
  int n = blockIdx.x*64 + threadIdx.x; int m = blockIdx.y;
  if(n>=N) return;
  float acc=0.f;
  for(int k=0;k<K;k++) acc += b2f(A[(size_t)m*K+k])*b2f(B[(size_t)k*N+n]);
  if(mode==1) acc = gelu_f(b2f(f2b(acc)));
  C[(size_t)m*N+n]=f2b(acc);
}
__global__ void ln_naive(const bf16* X, bf16* Y){
  int m = blockIdx.x; int t = threadIdx.x;
  float s=0; for(int k=t;k<D_;k+=64) s+=b2f(X[m*D_+k]);
  for(int o=32;o;o>>=1) s+=__shfl_xor(s,o);
  float mean=s/D_; float v=0; for(int k=t;k<D_;k+=64){float d=b2f(X[m*D_+k])-mean; v+=d*d;}
  for(int o=32;o;o>>=1) v+=__shfl_xor(v,o);
  float r=rsqrtf(v/D_+1e-5f);
  for(int k=t;k<D_;k+=64) Y[m*D_+k]=f2b((b2f(X[m*D_+k])-mean)*r);
}
__global__ void shift_naive(const bf16* Hh, bf16* G){
  int m = blockIdx.x;
  for(int k=threadIdx.x;k<D_;k+=64){
    int g=k/112; int s = (g==0)?1:(g==1)?-1:(g==2)?2:-2; int src=m-s;
    G[m*D_+k] = (src>=0&&src<S_)? Hh[src*D_+k] : f2b(0.f);
  }
}
__global__ void add_naive(const bf16* A, const bf16* B, bf16* C, int n){
  int i=blockIdx.x*256+threadIdx.x; if(i<n) C[i]=f2b(b2f(A[i])+b2f(B[i]));
}
__global__ void attn_naive(const bf16* QKV, bf16* O){
  int q = blockIdx.x; int h = blockIdx.y; int t=threadIdx.x;
  __shared__ float p[256]; __shared__ float red[4];
  int k = q - 255 + t; float sc=-1e30f;
  if(k>=0){ float a=0; for(int d=0;d<DH_;d++) a+=b2f(QKV[q*1344+h*DH_+d])*b2f(QKV[k*1344+448+h*DH_+d]); sc=a*0.1889822365046136f; }
  float m=sc; for(int o=32;o;o>>=1) m=fmaxf(m,__shfl_xor(m,o));
  if((t&63)==0) red[t>>6]=m; __syncthreads();
  m=fmaxf(fmaxf(red[0],red[1]),fmaxf(red[2],red[3])); __syncthreads();
  float e=(k>=0)?__expf(sc-m):0.f; float s=e; for(int o=32;o;o>>=1) s+=__shfl_xor(s,o);
  if((t&63)==0) red[t>>6]=s; __syncthreads();
  s=red[0]+red[1]+red[2]+red[3];
  p[t]=e/s; __syncthreads();
  if(t<DH_){ float a=0; for(int j=0;j<256;j++){ int kk=q-255+j; if(kk>=0) a+=p[j]*b2f(QKV[kk*1344+896+h*DH_+t]); } O[q*D_+h*DH_+t]=f2b(a);}
}
__global__ void final_naive(const bf16* X, const bf16* gw, const bf16* A, const bf16* X1, const bf16* H2, bf16* out){
  int m = blockIdx.x; int t=threadIdx.x;
  float s=0; for(int k=t;k<D_;k+=64) s+=b2f(X[m*D_+k])*b2f(gw[k]);
  for(int o=32;o;o>>=1) s+=__shfl_xor(s,o);
  float g=1.f/(1.f+__expf(-s));
  for(int k=t;k<D_;k+=64){ float b=b2f(X1[m*D_+k])+b2f(H2[m*D_+k]); out[m*D_+k]=f2b(g*b2f(A[m*D_+k])+(1.f-g)*b);}
}

extern "C" void kernel_launch(void* const* d_in, const int* in_sizes, int n_in,
                              void* d_out, int out_size, void* d_ws, size_t ws_size,
                              hipStream_t stream) {
  const bf16* x=(const bf16*)d_in[0]; const bf16* wqkv=(const bf16*)d_in[1]; const bf16* wo=(const bf16*)d_in[2];
  const bf16* w1=(const bf16*)d_in[4]; const bf16* w2=(const bf16*)d_in[5]; const bf16* w3=(const bf16*)d_in[6]; const bf16* w4=(const bf16*)d_in[7];
  const bf16* gw=(const bf16*)d_in[8];
  bf16* ws=(bf16*)d_ws; size_t SD=(size_t)S_*D_;
  bf16* qkv=ws; bf16* o=qkv+3*SD; bf16* a=o+SD; bf16* ln=a+SD; bf16* h1=ln+SD; bf16* g=h1+SD; bf16* hs=g+SD; bf16* x1=hs+SD; bf16* h2=x1+SD; bf16* y=h2+4*SD;
  gemm_naive<<<dim3(21,S_),64,0,stream>>>(x,wqkv,qkv,S_,1344,D_,0);
  attn_naive<<<dim3(S_,H_),256,0,stream>>>(qkv,o);
  gemm_naive<<<dim3(7,S_),64,0,stream>>>(o,wo,a,S_,D_,D_,0);
  ln_naive<<<S_,64,0,stream>>>(x,ln);
  gemm_naive<<<dim3(7,S_),64,0,stream>>>(ln,w1,h1,S_,D_,D_,1);
  shift_naive<<<S_,64,0,stream>>>(h1,g);
  gemm_naive<<<dim3(7,S_),64,0,stream>>>(g,w2,hs,S_,D_,D_,0);
  add_naive<<<(SD+255)/256,256,0,stream>>>(x,hs,x1,SD);
  ln_naive<<<S_,64,0,stream>>>(x1,ln);
  gemm_naive<<<dim3(28,S_),64,0,stream>>>(ln,w3,h2,S_,F_,D_,1);
  gemm_naive<<<dim3(7,S_),64,0,stream>>>(h2,w4,y,S_,D_,F_,0);
  final_naive<<<S_,64,0,stream>>>(x,gw,a,x1,y,(bf16*)d_out);
}
```

```cpp
#include <hip/hip_runtime.h>
#include <hip/hip_bf16.h>
#include <stdint.h>

typedef __attribute__((ext_vector_type(8))) __bf16 bf16x8;
typedef __attribute__((ext_vector_type(4))) __bf16 bf16x4;
typedef __attribute__((ext_vector_type(2))) __bf16 bf16x2;
typedef __attribute__((ext_vector_type(16))) float f32x16;
typedef __attribute__((ext_vector_type(4))) float f32x4;
typedef __attribute__((ext_vector_type(2))) float f32x2;
typedef __attribute__((ext_vector_type(4))) unsigned u32x4;
typedef __attribute__((ext_vector_type(2))) unsigned u32x2;
typedef unsigned short u16;
typedef unsigned long long u64;
#define LAS __attribute__((address_space(3)))
#define GAS __attribute__((address_space(1)))
#define DI __device__ __forceinline__
#define RLX_AGENT __ATOMIC_RELAXED, __HIP_MEMORY_SCOPE_AGENT

constexpr int S_ = 2048, D_ = 448, F_ = 1792;

constexpr unsigned WS_EPOCH = 0;
constexpr unsigned WS_FL = 256;
constexpr unsigned FL_P1 = 0, FL_X1 = 2048, FL_O = 3072, FL_H2 = 4096, FL_N = 6144;
constexpr unsigned WS_Q = 1u << 20;
constexpr unsigned WS_K = WS_Q + 16 * 64 * 2048;
constexpr unsigned WS_V = WS_K + 16 * 64 * 2048;
constexpr unsigned WS_H1 = WS_V + 16 * 64 * 2048;
constexpr unsigned WS_X1 = WS_H1 + 64 * 28 * 1024;
constexpr unsigned WS_ST = WS_X1 + 128 * 14 * 1024;
constexpr unsigned WS_O = WS_ST + 128 * 8 * 128;
constexpr unsigned WS_H2 = WS_O + 64 * 32 * 1024;
constexpr unsigned WS_G = WS_H2 + 64 * 112 * 1024;
constexpr unsigned WS_END = WS_G + 2048 * 4;

constexpr int L_W3 = 0;
constexpr int L_RED2 = 100352 + 256;
constexpr int L_CSF = L_RED2 + 49152;
constexpr int L_MISC = L_CSF + 7168;
constexpr int L_P1S = L_MISC + 256;
constexpr int LDS_BYTES = 160 * 1024;

#define STAMP(i)

#define MFMA32(a, b, c) __builtin_amdgcn_mfma_f32_32x32x16_bf16((a), (b), (c), 0, 0, 0)
#define MFMA16(a, b, c) __builtin_amdgcn_mfma_f32_16x16x32_bf16((a), (b), (c), 0, 0, 0)
#define WAIT_VM(n) asm volatile("s_waitcnt vmcnt(" #n ")" ::: "memory")
#define WAIT_LGKM0 asm volatile("s_waitcnt lgkmcnt(0)" ::: "memory")
#define BARRIER() do { asm volatile("s_waitcnt lgkmcnt(0)" ::: "memory"); __builtin_amdgcn_s_barrier(); asm volatile("" ::: "memory"); } while (0)

DI bf16x8 as_bf8(u32x4 v) { return __builtin_bit_cast(bf16x8, v); }
DI unsigned pk2(float lo, float hi) {
  bf16x2 r = {(__bf16)lo, (__bf16)hi};
  return __builtin_bit_cast(unsigned, r);
}
DI float bflo(unsigned u) { return __uint_as_float(u << 16); }
DI float bfhi(unsigned u) { return __uint_as_float(u & 0xffff0000u); }
DI float dot2(unsigned a, unsigned b, float c) {
  return __builtin_amdgcn_fdot2_f32_bf16(__builtin_bit_cast(bf16x2, a), __builtin_bit_cast(bf16x2, b), c, false);
}
DI float swap32_add(float v) {
  unsigned u = __float_as_uint(v);
  auto r = __builtin_amdgcn_permlane32_swap(u, u, false, false);
  return __uint_as_float(r[0]) + __uint_as_float(r[1]);
}
DI float swap16_add(float v) {
  unsigned u = __float_as_uint(v);
  auto r = __builtin_amdgcn_permlane16_swap(u, u, false, false);
  return __uint_as_float(r[0]) + __uint_as_float(r[1]);
}
DI float swap32_max(float v) {
  unsigned u = __float_as_uint(v);
  auto r = __builtin_amdgcn_permlane32_swap(u, u, false, false);
  return fmaxf(__uint_as_float(r[0]), __uint_as_float(r[1]));
}
DI float gelu_sig(float x) {
  const float c0 = -2.f * 0.7978845608028654f * 1.4426950408889634f;
  const float c1 = c0 * 0.044715f;
  float t = x * x;
  float p = __builtin_fmaf(t, c1, c0);
  float e = __builtin_amdgcn_exp2f(x * p);
  return __builtin_amdgcn_rcpf(1.f + e);
}
DI u32x4 pack8(const f32x16& a, int s) {
  u32x4 r;
  r[0] = pk2(a[8 * s + 0], a[8 * s + 1]);
  r[1] = pk2(a[8 * s + 2], a[8 * s + 3]);
  r[2] = pk2(a[8 * s + 4], a[8 * s + 5]);
  r[3] = pk2(a[8 * s + 6], a[8 * s + 7]);
  return r;
}
DI f32x16 splat16(float v) {
  f32x16 z;
#pragma unroll
  for (int i = 0; i < 16; ++i) z[i] = v;
  return z;
}
DI void dma16(const void* sbase, unsigned voff, unsigned ldst) {
  unsigned keep;
  asm volatile("s_mov_b32 %0, m0\n\ts_mov_b32 m0, %3\n\ts_nop 0\n\tglobal_load_lds_dwordx4 %1, %2\n\ts_mov_b32 m0, %0"
               : "=&s"(keep) : "v"(voff), "s"(sbase), "s"(ldst) : "memory");
}
DI bf16x8 tr_frag(const LAS char* p, int off2) {
  bf16x4 a = __builtin_amdgcn_ds_read_tr16_b64_v4bf16((LAS bf16x4*)(p));
  bf16x4 b = __builtin_amdgcn_ds_read_tr16_b64_v4bf16((LAS bf16x4*)(p + off2));
  return __builtin_shufflevector(a, b, 0, 1, 2, 3, 4, 5, 6, 7);
}
typedef __amdgpu_buffer_rsrc_t rsrc_t;
DI u32x4 ld16(rsrc_t rs, unsigned voff, unsigned soff) { return __builtin_amdgcn_raw_buffer_load_b128(rs, voff, soff, 16); }
DI u32x2 ld8(rsrc_t rs, unsigned voff, unsigned soff) { return __builtin_amdgcn_raw_buffer_load_b64(rs, voff, soff, 16); }
DI unsigned ld4(rsrc_t rs, unsigned voff, unsigned soff) { return __builtin_amdgcn_raw_buffer_load_b32(rs, voff, soff, 16); }
DI void st16(rsrc_t rs, u32x4 v, unsigned voff, unsigned soff) { __builtin_amdgcn_raw_buffer_store_b128(v, rs, voff + soff, 0, 16);    }
DI void st8(rsrc_t rs, u32x2 v, unsigned voff, unsigned soff) { __builtin_amdgcn_raw_buffer_store_b64(v, rs, voff, soff, 16); }
DI void st4(rsrc_t rs, unsigned v, unsigned voff, unsigned soff) { __builtin_amdgcn_raw_buffer_store_b32(v, rs, voff, soff, 16); }

DI void set_flag(char* ws, unsigned idx, u64 tag, int lane) {
  if (lane == 0) __hip_atomic_store((GAS u64*)(ws + WS_FL + (size_t)idx * 128), tag, RLX_AGENT);
}
DI void poll_flags(char* ws, int idx, u64 tag) {
  GAS u64* p = (GAS u64*)(ws + WS_FL + (size_t)max(idx, 0) * 128);
  for (int spins = 0; spins < (1 << 16); ++spins) {
    u64 v = __hip_atomic_load(p, RLX_AGENT);
    if (__all(idx < 0 || v == tag)) break;
    __builtin_amdgcn_s_sleep(1);
  }
  asm volatile("" ::: "memory");
}
DI void lds_arrive(LAS char* lds, int off, int lane) {
  WAIT_LGKM0;
  if (lane == 0) __hip_atomic_fetch_add((LAS unsigned*)(lds + off), 1u, __ATOMIC_RELAXED, __HIP_MEMORY_SCOPE_WORKGROUP);
}
DI void lds_wait(LAS char* lds, int off, unsigned n) {
  for (int spins = 0; spins < (1 << 20); ++spins) {
    unsigned v = __hip_atomic_load((LAS unsigned*)(lds + off), __ATOMIC_RELAXED, __HIP_MEMORY_SCOPE_WORKGROUP);
    if (__builtin_amdgcn_readfirstlane(v) >= n) break;
    __builtin_amdgcn_s_sleep(0);
  }
  asm volatile("" ::: "memory");
}
DI int fl_p1(int rt, int ct, int wv) { return FL_P1 + (rt * 16 + ct) * 8 + wv; }

struct Params {
  const u16 *x, *wqkv, *wo, *w1, *w2, *w3, *w4, *gw;
  char* ws;
  u16* out;
};

__global__ __launch_bounds__(512, 2) void mega_kernel(Params P) {
  extern __shared__ __attribute__((aligned(16))) char lds_raw[];
  LAS char* lds = (LAS char*)lds_raw;
  const unsigned lds0 = (unsigned)(size_t)lds;
  char* ws = P.ws;
  const int tid = threadIdx.x, lane = tid & 63, wave = __builtin_amdgcn_readfirstlane(tid >> 6);
  const int r = lane & 31, h = lane >> 5;
  const int b = blockIdx.x, xcd = b & 7, idx = b >> 3;
  const int rt = (xcd >> 1) * 4 + (idx & 3);
  const int ct = (xcd & 1) * 8 + (idx >> 2);
  const rsrc_t wsr = __builtin_amdgcn_make_buffer_rsrc(ws, 0, WS_END, 0x00020000);
  STAMP(0);

  u64 tag;
  {
    const int type = ct >> 2;
    const int w4 = wave & 3, ch = wave >> 2;
    const int strip = rt * 4 + w4;
    int shift = 0;
    if (type == 3) { int g = ct & 3; shift = (g == 0) ? 1 : (g == 1) ? -1 : (g == 2) ? 2 : -2; }
    const int rot = (2 * (rt & 3) + ct) % 7;
    const u16* W = (type == 3) ? (P.w1 + (ct & 3) * 112) : (P.wqkv + ct * 112);
    const unsigned ldb = (type == 3) ? 896u : 2688u;
    unsigned xoff[2], woff[2];
    bool wact;
#pragma unroll
    for (int i = 0; i < 2; ++i) {
      int row = 16 * wave + 8 * i + (lane >> 3);
      int tok = min(max(rt * 128 + row - shift, 0), S_ - 1);
      xoff[i] = (unsigned)tok * 896u + (unsigned)(((lane & 7) ^ ((row >> 1) & 7)) * 16);
      int k = 8 * wave + 4 * i + (lane >> 4);
      int sg = (lane & 15) ^ ((k & 3) << 2);
      woff[i] = (unsigned)k * ldb + (unsigned)sg * 16u;
      wact = sg < 14;
    }
    u64 ep;
    unsigned gv[7];
    {
      asm volatile("global_load_dwordx2 %0, %1, %2 sc1" : "=v"(ep) : "v"(0u), "s"(ws + WS_EPOCH) : "memory");
#pragma unroll
      for (int c = 0; c < 7; ++c) {
        int cc = c + rot; cc = cc >= 7 ? cc - 7 : cc;
        asm volatile("global_load_ushort %0, %1, %2" : "=v"(gv[c]) : "v"((unsigned)(lane * 2)), "s"(P.gw + cc * 64) : "memory");
      }
    }
    auto issue = [&](int c) {
      int cc = c + rot; cc = cc >= 7 ? cc - 7 : cc;
      const char* xb = (const char*)P.x + cc * 128;
      const char* wb = (const char*)W + (size_t)cc * 64 * ldb;
      const unsigned sl = lds0 + (c & 3) * 32768;
      dma16(xb, xoff[0], sl + wave * 2048);
      dma16(xb, xoff[1], sl + wave * 2048 + 1024);
      if (wact) {
        dma16(wb, woff[0], sl + 16384 + wave * 2048);
        dma16(wb, woff[1], sl + 16384 + wave * 2048 + 1024);
      }
    };
    issue(0); issue(1); issue(2);
    if (tid < 16) *(LAS unsigned*)(lds + L_MISC + tid * 4) = 0u;
    if (tid < 128) *(LAS float*)(lds + L_P1S + tid * 4) = 0.f;

    const int blk = (lane >> 4) & 1, q = (lane & 15) >> 2, pp = lane & 3;
    const int ustride = (type == 3) ? 64 : 56;
    const LAS char* wa[2];
#pragma unroll
    for (int i = 0; i < 2; ++i) {
      int bo = ustride * (2 * ch + i) + 32 * blk + 8 * pp;
      wa[i] = lds + 16384 + (8 * h + q) * 256 + (((bo >> 6) ^ q) << 6) + (bo & 63);
    }
    const LAS char* xa[4];
#pragma unroll
    for (int s4 = 0; s4 < 4; ++s4) xa[s4] = lds + (w4 * 32 + r) * 128 + (((2 * s4 + h) ^ ((r >> 1) & 7)) * 16);
    const bool do_aux = (wave == 0) && (type == 3 || ct == 4);
    LAS char* auxp = lds + 16384 + lane * 256 + ((14 ^ ((lane & 3) << 2)) * 16);

    f32x16 acc[2], gram;
    acc[0] = splat16(0.f); acc[1] = splat16(0.f); gram = splat16(0.f);
    float cs0 = 0.f, cs1 = 0.f;

#pragma unroll
    for (int c = 0; c < 7; ++c) {
      if (c <= 4) { WAIT_VM(8); } else if (c == 5) { WAIT_VM(4); } else { WAIT_VM(0); }
      if (c == 0) asm volatile("" : "+v"(ep), "+v"(gv[0]), "+v"(gv[1]), "+v"(gv[2]), "+v"(gv[3]), "+v"(gv[4]), "+v"(gv[5]), "+v"(gv[6]));
      if (do_aux) *(LAS u16*)(auxp + (c & 3) * 32768) = (type == 3) ? (u16)0x3F80 : (u16)gv[c];
      BARRIER();
      if (c == 0) STAMP(1);
      if (c + 3 < 7) issue(c + 3);
      const int so = (c & 3) * 32768;
#pragma unroll
      for (int s4 = 0; s4 < 4; ++s4) {
        u32x4 xf = *(const LAS u32x4*)(xa[s4] + so);
        bf16x8 wf0 = tr_frag(wa[0] + so + s4 * 4096, 1024);
        bf16x8 wf1 = tr_frag(wa[1] + so + s4 * 4096, 1024);
        if (type == 2) {
          acc[0] = MFMA32(as_bf8(xf), wf0, acc[0]);
          acc[1] = MFMA32(as_bf8(xf), wf1, acc[1]);
        } else {
          acc[0] = MFMA32(wf0, as_bf8(xf), acc[0]);
          acc[1] = MFMA32(wf1, as_bf8(xf), acc[1]);
        }
        if (type == 3) {
          if ((s4 >> 1) == ch) gram = MFMA32(as_bf8(xf), as_bf8(xf), gram);
          if (s4 == w4) {
            u32x4 a0 = __builtin_bit_cast(u32x4, wf0), a1 = __builtin_bit_cast(u32x4, wf1);
#pragma unroll
            for (int d = 0; d < 4; ++d) { cs0 = dot2(a0[d], 0x3F803F80u, cs0); cs1 = dot2(a1[d], 0x3F803F80u, cs1); }
          }
        }
      }
    }
    STAMP(2);
    tag = __builtin_amdgcn_readfirstlane((unsigned)ep) | ((u64)__builtin_amdgcn_readfirstlane((unsigned)(ep >> 32)) << 32);
    tag += 1;

    if (type == 2) {
#pragma unroll
      for (int i = 0; i < 2; ++i) {
        int head = (ct & 3) * 4 + 2 * ch + i;
#pragma unroll
        for (int s = 0; s < 2; ++s) {
          u32x4 v = pack8(acc[i], s);
          if (r >= 28) { unsigned cst = (r == 28) ? 0x3F803F80u : 0u; v[0] = cst; v[1] = cst; v[2] = cst; v[3] = cst; }
          st16(wsr, v, lane * 16, WS_V + ((head * 64 + strip) * 2 + s) * 1024);
        }
      }
      BARRIER();
    } else if (type < 2) {
      if (ct == 4 && ch == 1) {
        float glog = acc[1][12];
        float g = __builtin_amdgcn_rcpf(1.f + __builtin_amdgcn_exp2f(-1.4426950408889634f * glog));
        if (h == 1) st4(wsr, __float_as_uint(g), r * 4, WS_G + strip * 128);
      }
      const float sc = (type == 0) ? (0.1889822365046136f * 1.4426950408889634f) : 1.f;
      const unsigned dst = (type == 0 ? WS_Q : WS_K);
#pragma unroll
      for (int i = 0; i < 2; ++i) {
        int head = (ct & 3) * 4 + 2 * ch + i;
#pragma unroll
        for (int k = 0; k < 16; ++k) acc[i][k] *= sc;
#pragma unroll
        for (int s = 0; s < 2; ++s) {
          u32x4 v = pack8(acc[i], s);
          if (s == 1 && h == 1) { v[2] = 0u; v[3] = 0u; }
          st16(wsr, v, lane * 16, dst + ((head * 64 + strip) * 2 + s) * 1024);
        }
      }
      BARRIER();
    } else {
      LAS float* csl = (LAS float*)(lds + L_P1S);
      LAS float* sts = (LAS float*)(lds + L_P1S + 512);
      cs0 = swap32_add(cs0); cs1 = swap32_add(cs1);
      __hip_atomic_fetch_add(csl + (2 * ch + h) * 32 + r, h ? cs1 : cs0, __ATOMIC_RELAXED, __HIP_MEMORY_SCOPE_WORKGROUP);
      float dg = 0.f;
      const int dreg = (r & 3) + 4 * (r >> 3);
#pragma unroll
      for (int k = 0; k < 16; ++k) dg = (dreg == k) ? gram[k] : dg;
      if (h == ((r >> 2) & 1)) sts[(w4 * 3 + ch) * 32 + r] = dg;
      if (ch == 1 && h == 0) sts[(w4 * 3 + 2) * 32 + r] = acc[1][8];
      BARRIER();
      const float sxx = sts[(w4 * 3 + 0) * 32 + r] + sts[(w4 * 3 + 1) * 32 + r];
      const float sx = sts[(w4 * 3 + 2) * 32 + r];
      const float mean = sx * (1.f / 448.f);
      const float var = fmaxf(sxx * (1.f / 448.f) - mean * mean, 0.f);
      const float rstd = __builtin_amdgcn_rsqf(var + 1e-5f);
      u32x4 bm = {0u, 0u, 0u, 0u};
      {
        __bf16 mh = (__bf16)(-mean);
        float ml = -mean - (float)mh;
        bm[0] = h ? 0u : pk2((float)mh, (float)mh);
        bm[1] = h ? 0u : pk2(ml, 0.f);
      }
#pragma unroll
      for (int i = 0; i < 2; ++i) {
        float c = csl[(2 * ch + i) * 32 + r];
        __bf16 chh = (__bf16)c;
        float cl = c - (float)chh;
        u32x4 am = {0u, 0u, 0u, 0u};
        am[0] = h ? 0u : pk2((float)chh, cl);
        am[1] = h ? 0u : pk2((float)chh, 0.f);
        acc[i] = MFMA32(as_bf8(am), as_bf8(bm), acc[i]);
      }
      const int tok = strip * 32 + r - shift;
      const float rs = (tok >= 0 && tok < S_) ? rstd : 0.f;
      const unsigned dst = WS_H1 + ((strip * 4 + (ct & 3)) * 7 + 4 * ch) * 1024;
#pragma unroll
      for (int f = 0; f < 4; ++f) {
        if (f == 3 && ch == 1) break;
        const int i = f >> 1, s = f & 1;
        float y[8];
#pragma unroll
        for (int j = 0; j < 8; ++j) {
          float xv = acc[i][8 * s + j] * rs;
          y[j] = xv * gelu_sig(xv);
        }
        u32x4 v = {pk2(y[0], y[1]), pk2(y[2], y[3]), pk2(y[4], y[5]), pk2(y[6], y[7])};
        st16(wsr, v, lane * 16, dst + f * 1024);
      }
    }
    STAMP(3);
    if (wave < 7) {
#pragma unroll
      for (int t = 0; t < 7; ++t) {
        int p = 64 * t + lane;
        int pi = p / 14, c = p - pi * 14;
        int k = (pi & ~15) + 8 * ((pi >> 2) & 1) + 4 * ((pi >> 3) & 1) + (pi & 3);
        unsigned off = (unsigned)(k * 3584 + 16 * c);
#pragma unroll
        for (int gi = 0; gi < 2; ++gi) {
          int grp = wave * 2 + gi;
          dma16((const char*)(P.w3 + ct * 112) + (size_t)grp * 32 * 3584, off, lds0 + L_W3 + grp * 7168 + t * 1024);
        }
      }
      WAIT_VM(14);
    } else {
      WAIT_VM(0);
    }
    set_flag(ws, fl_p1(rt, ct, wave), tag, lane);
    STAMP(4);
  }

  const bool hasB = b < 224;
  const int mt = xcd * 4 + (idx & 3);
  const int nt = idx >> 2;

  if (wave < 4) {
    if (hasB) {
      const int g = wave;
      const u16* wp = P.w2 + (size_t)(112 * g + 4 * h) * D_ + 64 * nt + 2 * r;
      unsigned wr[7][8];
#pragma unroll
      for (int f = 0; f < 7; ++f)
#pragma unroll
        for (int j = 0; j < 8; ++j) wr[f][j] = *(const unsigned*)(wp + (size_t)(16 * f + 8 * (j >> 2) + (j & 3)) * D_);
      u32x4 xr[4];
      {
        const u16* xp = P.x + (size_t)(64 * mt + 32 * (g >> 1) + r) * D_ + 64 * nt + 8 * h;
#pragma unroll
        for (int a = 0; a < 4; ++a) xr[a] = *(const u32x4*)(xp + 16 * a);
      }
      poll_flags(ws, lane < 4 ? fl_p1(mt >> 1, 12 + g, ((2 * mt + (lane & 1)) & 3) + 4 * (lane >> 1)) : -1, tag);
      STAMP(5);
      lds_arrive(lds, L_MISC + 0, lane);
      u32x4 hf[2][7];
#pragma unroll
      for (int t = 0; t < 2; ++t)
#pragma unroll
        for (int f = 0; f < 7; ++f) hf[t][f] = ld16(wsr, lane * 16, WS_H1 + (((2 * mt + t) * 4 + g) * 7 + f) * 1024);
      f32x16 acc[2][2];
#pragma unroll
      for (int i = 0; i < 2; ++i)
#pragma unroll
        for (int t = 0; t < 2; ++t) acc[i][t] = splat16(0.f);
#pragma unroll
      for (int t = 0; t < 2; ++t)
#pragma unroll
        for (int f = 0; f < 7; ++f) {
          u32x4 a0, a1;
#pragma unroll
          for (int d = 0; d < 4; ++d) {
            a0[d] = __builtin_amdgcn_perm(wr[f][2 * d + 1], wr[f][2 * d], 0x05040100u);
            a1[d] = __builtin_amdgcn_perm(wr[f][2 * d + 1], wr[f][2 * d], 0x07060302u);
          }
          acc[0][t] = MFMA32(as_bf8(a0), as_bf8(hf[t][f]), acc[0][t]);
          acc[1][t] = MFMA32(as_bf8(a1), as_bf8(hf[t][f]), acc[1][t]);
        }
      STAMP(6);
      const int l32 = (r & 15) + 16 * h;
#pragma unroll
      for (int t = 0; t < 2; ++t) {
        const int o = 2 * t + (r >> 4);
        const int si = (g - o - 1) & 3;
        if (o != g) {
          LAS char* p = lds + L_RED2 + ((o * 3 + si) * 8) * 512 + l32 * 16;
#pragma unroll
          for (int i = 0; i < 2; ++i)
#pragma unroll
            for (int a = 0; a < 4; ++a) {
              const f32x16& A = acc[i][t];
              *(LAS f32x4*)(p + (i * 4 + a) * 512) = f32x4{A[4 * a], A[4 * a + 1], A[4 * a + 2], A[4 * a + 3]};
            }
        }
      }
      lds_arrive(lds, L_MISC + 4, lane);
      lds_wait(lds, L_MISC + 4, 4);
      {
        f32x16 o0 = (g >> 1) ? acc[0][1] : acc[0][0];
        f32x16 o1 = (g >> 1) ? acc[1][1] : acc[1][0];
        const LAS char* p = lds + L_RED2 + (g * 3 * 8) * 512 + l32 * 16;
#pragma unroll
        for (int si = 0; si < 3; ++si)
#pragma unroll
          for (int a = 0; a < 4; ++a) {
            f32x4 u0 = *(const LAS f32x4*)(p + (si * 8 + a) * 512);
            f32x4 u1 = *(const LAS f32x4*)(p + (si * 8 + 4 + a) * 512);
#pragma unroll
            for (int k = 0; k < 4; ++k) { o0[4 * a + k] += u0[k]; o1[4 * a + k] += u1[k]; }
          }
        float sx = 0.f, sxx = 0.f;
        const bool own = (r >> 4) == (g & 1);
        const int s16 = 4 * mt + g;
#pragma unroll
        for (int a = 0; a < 4; ++a) {
          u32x4 v;
#pragma unroll
          for (int k = 0; k < 4; ++k) {
            v[k] = pk2(o0[4 * a + k] + bflo(xr[a][k]), o1[4 * a + k] + bfhi(xr[a][k]));
            sx = dot2(v[k], 0x3F803F80u, sx);
            sxx = dot2(v[k], v[k], sxx);
          }
          if (own) st16(wsr, v, ((r & 15) + 16 * (2 * (a & 1) + h)) * 16, WS_X1 + (s16 * 14 + 2 * nt + (a >> 1)) * 1024);
        }
        sx = swap32_add(sx); sxx = swap32_add(sxx);
        if (own && h == 0) st8(wsr, u32x2{__float_as_uint(sx), __float_as_uint(sxx)}, (r & 15) * 8, WS_ST + (s16 * 8 + nt) * 128);
      }
      WAIT_VM(0);
      set_flag(ws, FL_X1 + (mt * 7 + nt) * 4 + g, tag, lane);
      STAMP(7);
    } else {
      WAIT_VM(0);
      lds_arrive(lds, L_MISC + 0, lane);
    }
    lds_wait(lds, L_MISC + 0, 8);
    {
      const int q4 = lane >> 4, q = (lane & 15) >> 2, pp = lane & 3;
      const LAS char* ab = lds + L_W3 + (16 * (q4 >> 1) + 4 * (q4 & 1) + q) * 224 + 8 * pp;
#pragma unroll
      for (int ti = 0; ti < 2; ++ti) {
        const int tau = wave + 4 * ti;
        if (tau < 7) {
          float c0 = 0.f, c1 = 0.f;
#pragma unroll
          for (int s = 0; s < 14; ++s) {
            u32x4 a = __builtin_bit_cast(u32x4, tr_frag(ab + s * 7168 + tau * 32, 1792));
            c0 = dot2(a[0], 0x3F803F80u, c0); c1 = dot2(a[1], 0x3F803F80u, c1);
            c0 = dot2(a[2], 0x3F803F80u, c0); c1 = dot2(a[3], 0x3F803F80u, c1);
          }
          float c = swap16_add(swap32_add(c0 + c1));
          __bf16 chh = (__bf16)c;
          float cl = c - (float)chh;
          u32x4 am = {0u, 0u, 0u, 0u};
          am[0] = q4 ? 0u : pk2((float)chh, cl);
          am[1] = q4 ? 0u : pk2((float)chh, 0.f);
          *(LAS u32x4*)(lds + L_CSF + tau * 1024 + lane * 16) = am;
        }
      }
    }
    STAMP(8);
  } else {
    const int head = 2 * xcd + (idx & 1), qs = (idx >> 1) * 4 + (wave - 4);
    {
      int fi = -1;
      const int hw = ((head & 3) >> 1) * 4;
      if (lane < 18) {
        int ks = qs - 8 + (lane % 9);
        if (ks >= 0) fi = fl_p1(ks >> 2, (lane < 9 ? 4 : 8) + (head >> 2), (ks & 3) + hw);
      } else if (lane == 18) fi = fl_p1(qs >> 2, head >> 2, (qs & 3) + hw);
      else if (lane == 19) fi = fl_p1(qs >> 2, 4, (qs & 3) + 4);
      poll_flags(ws, fi, tag);
    }
    STAMP(5);
    lds_arrive(lds, L_MISC + 0, lane);
    u32x4 qf0 = ld16(wsr, lane * 16, WS_Q + ((head * 64 + qs) * 2) * 1024);
    u32x4 qf1 = ld16(wsr, lane * 16, WS_Q + ((head * 64 + qs) * 2 + 1) * 1024);
    u32x4 kf[9][2], vf[9][2];
#pragma unroll
    for (int t = 0; t < 9; ++t) {
      int ks = max(qs - 8 + t, 0);
      kf[t][0] = ld16(wsr, lane * 16, WS_K + ((head * 64 + ks) * 2) * 1024);
      kf[t][1] = ld16(wsr, lane * 16, WS_K + ((head * 64 + ks) * 2 + 1) * 1024);
    }
#pragma unroll
    for (int t = 0; t < 9; ++t) {
      int ks = max(qs - 8 + t, 0);
      vf[t][0] = ld16(wsr, lane * 16, WS_V + ((head * 64 + ks) * 2) * 1024);
      vf[t][1] = ld16(wsr, lane * 16, WS_V + ((head * 64 + ks) * 2 + 1) * 1024);
    }
    const float gq = __uint_as_float(ld4(wsr, r * 4, WS_G + qs * 128));
    const float NEG = -1e30f;
    float m = NEG;
#pragma unroll
    for (int t = 0; t < 9; ++t) {
      if (qs - 8 + t >= 0) {
        f32x16 s = MFMA32(as_bf8(kf[t][0]), as_bf8(qf0), splat16(0.f));
        s = MFMA32(as_bf8(kf[t][1]), as_bf8(qf1), s);
#pragma unroll
        for (int i = 0; i < 16; ++i) {
          const int key = (i & 3) + 8 * (i >> 2) + 4 * h;
          float v = s[i];
          if (t == 0) v = (key > r) ? v : NEG;
          if (t == 8) v = (key <= r) ? v : NEG;
          m = fmaxf(m, v);
        }
      }
    }
    m = swap32_max(m);
    const f32x16 negm = splat16(-m);
    f32x16 o = splat16(0.f);
#pragma unroll
    for (int t = 0; t < 9; ++t) {
      if (qs - 8 + t >= 0) {
        f32x16 s = MFMA32(as_bf8(kf[t][0]), as_bf8(qf0), negm);
        s = MFMA32(as_bf8(kf[t][1]), as_bf8(qf1), s);
#pragma unroll
        for (int i = 0; i < 16; ++i) {
          const int key = (i & 3) + 8 * (i >> 2) + 4 * h;
          float v = __builtin_amdgcn_exp2f(s[i]);
          if (t == 0) v = (key > r) ? v : 0.f;
          if (t == 8) v = (key <= r) ? v : 0.f;
          s[i] = v;
        }
        o = MFMA32(as_bf8(vf[t][0]), as_bf8(pack8(s, 0)), o);
        o = MFMA32(as_bf8(vf[t][1]), as_bf8(pack8(s, 1)), o);
      }
    }
    float l = (h == 1) ? o[12] : 0.f;
    l = swap32_add(l);
    const float f = gq * __builtin_amdgcn_rcpf(l);
#pragma unroll
    for (int i = 0; i < 16; ++i) o[i] *= f;
    u32x4 o0 = pack8(o, 0), o1 = pack8(o, 1);
    if (h == 1) { o1[2] = 0u; o1[3] = 0u; }
    st16(wsr, o0, lane * 16, WS_O + ((qs * 16 + head) * 2) * 1024);
    st16(wsr, o1, lane * 16, WS_O + ((qs * 16 + head) * 2 + 1) * 1024);
    WAIT_VM(0);
    set_flag(ws, FL_O + qs * 16 + head, tag, lane);
    STAMP(7);
    lds_wait(lds, L_MISC + 0, 8);
    STAMP(8);
  }

  unsigned w4r[14][8];
  {
    const int s16 = rt * 8 + wave;
    const int t16 = lane & 15, q4 = lane >> 4, q = (lane & 15) >> 2, pp = lane & 3;
    poll_flags(ws, lane < 7 ? (int)(FL_X1 + ((s16 >> 2) * 7 + lane) * 4 + (s16 & 3)) : (lane == 7 ? fl_p1(rt, 4, (wave >> 1) + 4) : -1), tag);
    STAMP(9);
    u32x4 xf[14];
#pragma unroll
    for (int s = 0; s < 14; ++s) xf[s] = ld16(wsr, lane * 16, WS_X1 + (s16 * 14 + s) * 1024);
    u32x2 st0 = ld8(wsr, q4 * 128 + t16 * 8, WS_ST + s16 * 1024);
    u32x2 st1 = ld8(wsr, min(q4 + 4, 6) * 128 + t16 * 8, WS_ST + s16 * 1024);
    const float g = __uint_as_float(ld4(wsr, t16 * 4, WS_G + s16 * 64));
    const u16* wp0 = P.w4 + (size_t)(224 * wave + 4 * h) * D_ + 64 * min(nt, 6) + 2 * r;
    const u16* wp1 = P.w4 + (size_t)(224 * wave + 8 * h) * D_ + 64 * min(nt, 6) + 2 * r;
    const LAS char* ab = lds + L_W3 + (16 * (q4 >> 1) + 4 * (q4 & 1) + q) * 224 + 8 * pp;
    f32x4 acc[7];
#pragma unroll
    for (int tau = 0; tau < 7; ++tau) acc[tau] = f32x4{0.f, 0.f, 0.f, 0.f};
#pragma unroll
    for (int s = 0; s < 14; ++s) {
      {
        const int ks = s, c2 = ks / 7, kp = ks % 7;
#pragma unroll
        for (int j = 0; j < 8; ++j) {
          if (kp < 6) w4r[ks][j] = *(const unsigned*)(wp0 + (size_t)(112 * c2 + 32 * (kp >> 1) + 16 * (j >> 2) + 8 * (kp & 1) + (j & 3)) * D_);
          else w4r[ks][j] = *(const unsigned*)(wp1 + (size_t)(112 * c2 + 96 + j) * D_);
        }
      }
#pragma unroll
      for (int tau = 0; tau < 7; ++tau) {
        bf16x8 wf = tr_frag(ab + s * 7168 + tau * 32, 1792);
        acc[tau] = MFMA16(wf, as_bf8(xf[s]), acc[tau]);
      }
      __builtin_amdgcn_sched_barrier(0);
    }
    STAMP(10);
    float sx = __uint_as_float(st0[0]) + (q4 < 3 ? __uint_as_float(st1[0]) : 0.f);
    float sxx = __uint_as_float(st0[1]) + (q4 < 3 ? __uint_as_float(st1[1]) : 0.f);
    sx = swap16_add(swap32_add(sx));
    sxx = swap16_add(swap32_add(sxx));
    const float mean = sx * (1.f / 448.f);
    const float var = fmaxf(sxx * (1.f / 448.f) - mean * mean, 0.f);
    const float rstd = __builtin_amdgcn_rsqf(var + 1e-5f);
    u32x4 bm = {0u, 0u, 0u, 0u};
    {
      __bf16 mh = (__bf16)(-mean);
      float ml = -mean - (float)mh;
      bm[0] = q4 ? 0u : pk2((float)mh, (float)mh);
      bm[1] = q4 ? 0u : pk2(ml, 0.f);
    }
    BARRIER();
#pragma unroll
    for (int tau = 0; tau < 7; ++tau) {
      u32x4 am = *(const LAS u32x4*)(lds + L_CSF + tau * 1024 + lane * 16);
      acc[tau] = MFMA16(as_bf8(am), as_bf8(bm), acc[tau]);
    }
    const float rg = rstd * (1.f - g);
    unsigned yy[7][2];
#pragma unroll
    for (int tau = 0; tau < 7; ++tau) {
      float y[4];
#pragma unroll
      for (int k = 0; k < 4; ++k) {
        float a = acc[tau][k];
        y[k] = (a * rg) * gelu_sig(a * rstd);
      }
      yy[tau][0] = pk2(y[0], y[1]); yy[tau][1] = pk2(y[2], y[3]);
    }
    const unsigned dst = WS_H2 + (((s16 >> 1) * 16 + ct) * 7) * 1024;
    const int tr = 16 * (s16 & 1) + t16;
#pragma unroll
    for (int i = 0; i < 3; ++i) {
      u32x4 v = {yy[2 * i][0], yy[2 * i][1], yy[2 * i + 1][0], yy[2 * i + 1][1]};
      st16(wsr, v, (q4 >> 1) * 1024 + ((q4 & 1) * 32 + tr) * 16, dst + 2 * i * 1024);
    }
    {
      auto r0 = __builtin_amdgcn_permlane16_swap(yy[6][0], yy[6][0], false, false);
      auto r1 = __builtin_amdgcn_permlane16_swap(yy[6][1], yy[6][1], false, false);
      u32x4 v = {yy[6][0], yy[6][1], r0[1], r1[1]};
      if (!(q4 & 1)) st16(wsr, v, ((q4 >> 1) * 32 + tr) * 16, dst + 6 * 1024);
    }
    STAMP(11);
    WAIT_VM(0);
    set_flag(ws, FL_H2 + (rt * 16 + ct) * 8 + wave, tag, lane);
    STAMP(12);
  }

  if (hasB) {
    f32x16 acc[2][2];
#pragma unroll
    for (int i = 0; i < 2; ++i)
#pragma unroll
      for (int t = 0; t < 2; ++t) acc[i][t] = splat16(0.f);
    {
      const u16* wp = P.wo + 64 * nt + 2 * r;
      unsigned wr[4][8];
#pragma unroll
      for (int ks = 0; ks < 4; ++ks) {
        const int head = 2 * wave + (ks >> 1), s = ks & 1;
#pragma unroll
        for (int j = 0; j < 8; ++j) {
          int d = min(16 * s + 8 * (j >> 2) + 4 * h + (j & 3), 27);
          wr[ks][j] = *(const unsigned*)(wp + (size_t)(head * 28 + d) * D_);
        }
      }
      poll_flags(ws, lane < 4 ? (int)(FL_O + (2 * mt + (lane & 1)) * 16 + 2 * wave + (lane >> 1)) : -1, tag);
      u32x4 of[2][4];
#pragma unroll
      for (int t = 0; t < 2; ++t)
#pragma unroll
        for (int ks = 0; ks < 4; ++ks) of[t][ks] = ld16(wsr, lane * 16, WS_O + (((2 * mt + t) * 16 + 2 * wave) * 2 + ks) * 1024);
#pragma unroll
      for (int ks = 0; ks < 4; ++ks) {
        u32x4 a0, a1;
#pragma unroll
        for (int d = 0; d < 4; ++d) {
          a0[d] = __builtin_amdgcn_perm(wr[ks][2 * d + 1], wr[ks][2 * d], 0x05040100u);
          a1[d] = __builtin_amdgcn_perm(wr[ks][2 * d + 1], wr[ks][2 * d], 0x07060302u);
        }
#pragma unroll
        for (int t = 0; t < 2; ++t) {
          acc[0][t] = MFMA32(as_bf8(a0), as_bf8(of[t][ks]), acc[0][t]);
          acc[1][t] = MFMA32(as_bf8(a1), as_bf8(of[t][ks]), acc[1][t]);
        }
      }
    }
    u32x4 wa[14][2];
#pragma unroll
    for (int ks = 0; ks < 14; ++ks)
#pragma unroll
      for (int d = 0; d < 4; ++d) {
        wa[ks][0][d] = __builtin_amdgcn_perm(w4r[ks][2 * d + 1], w4r[ks][2 * d], 0x05040100u);
        wa[ks][1][d] = __builtin_amdgcn_perm(w4r[ks][2 * d + 1], w4r[ks][2 * d], 0x07060302u);
      }
    const int ts = wave >> 2, a_own = wave & 3;
    const int tok = 64 * mt + 32 * ts + r;
    u32x4 x1v = ld16(wsr, ((r & 15) + 16 * (2 * (a_own & 1) + h)) * 16 + (r >> 4) * 14 * 1024,
                     WS_X1 + ((4 * mt + 2 * ts) * 14 + 2 * nt + (a_own >> 1)) * 1024);
    const float og = 1.f - __uint_as_float(ld4(wsr, r * 4, WS_G + (2 * mt + ts) * 128));
    STAMP(13);
    poll_flags(ws, lane < 8 ? (int)(FL_H2 + ((mt >> 1) * 16 + 2 * wave + (lane & 1)) * 8 + 4 * (mt & 1) + (lane >> 1)) : -1, tag);
    STAMP(14);
    constexpr int WIN = 11;
    u32x4 hf[28];
    const unsigned h2base = WS_H2 + (((2 * mt) * 16 + 2 * wave) * 7) * 1024;
#pragma unroll
    for (int i = 0; i < WIN; ++i) hf[i] = ld16(wsr, lane * 16, h2base + (i / 14) * 112 * 1024 + (i % 14) * 1024);
#pragma unroll
    for (int t = 0; t < 2; ++t) {
#pragma unroll
      for (int ks = 0; ks < 14; ++ks) {
        acc[0][t] = MFMA32(as_bf8(wa[ks][0]), as_bf8(hf[14 * t + ks]), acc[0][t]);
        acc[1][t] = MFMA32(as_bf8(wa[ks][1]), as_bf8(hf[14 * t + ks]), acc[1][t]);
        const int i = 14 * t + ks + WIN;
        if (i < 28) {
          hf[i] = ld16(wsr, lane * 16, h2base + (i / 14) * 112 * 1024 + (i % 14) * 1024);
          __builtin_amdgcn_sched_barrier(0);
        }
      }
#pragma unroll
      for (int a = 0; a < 4; ++a) {
        const int o = 4 * t + a;
        if (o != wave) {
          const int si = (wave - o - 1) & 7;
#pragma unroll
          for (int i = 0; i < 2; ++i) {
            const f32x16& A = acc[i][t];
            *(LAS f32x4*)(lds + ((o * 7 + si) * 2 + i) * 1024 + lane * 16) = f32x4{A[4 * a], A[4 * a + 1], A[4 * a + 2], A[4 * a + 3]};
          }
        }
      }
    }
    STAMP(15);
    BARRIER();
    f32x4 fin[2];
#pragma unroll
    for (int i = 0; i < 2; ++i) {
      f32x16 A = ts ? acc[i][1] : acc[i][0];
      f32x4 own;
      if (a_own == 0) own = f32x4{A[0], A[1], A[2], A[3]};
      else if (a_own == 1) own = f32x4{A[4], A[5], A[6], A[7]};
      else if (a_own == 2) own = f32x4{A[8], A[9], A[10], A[11]};
      else own = f32x4{A[12], A[13], A[14], A[15]};
#pragma unroll
      for (int si = 0; si < 7; ++si) own += *(const LAS f32x4*)(lds + ((wave * 7 + si) * 2 + i) * 1024 + lane * 16);
      fin[i] = own;
    }
    u32x4 o;
#pragma unroll
    for (int k = 0; k < 4; ++k)
      o[k] = pk2(__builtin_fmaf(og, bflo(x1v[k]), fin[0][k]), __builtin_fmaf(og, bfhi(x1v[k]), fin[1][k]));
    *(u32x4*)(P.out + (size_t)tok * D_ + 64 * nt + 16 * a_own + 8 * h) = o;
    STAMP(16);
  } else if (b == 255 && wave == 0) {
#pragma unroll 1
    for (int i = 0; i < 4; ++i) poll_flags(ws, FL_P1 + (i * 64 + lane) * 8, tag);
    if (lane == 0) __hip_atomic_store((GAS u64*)(ws + WS_EPOCH), tag, RLX_AGENT);
  }
}

extern "C" void kernel_launch(void* const* d_in, const int* in_sizes, int n_in, void* d_out, int out_size, void* d_ws,
                              size_t ws_size, hipStream_t stream) {
  Params P;
  P.x = (const u16*)d_in[0];
  P.wqkv = (const u16*)d_in[1];
  P.wo = (const u16*)d_in[2];
  P.w1 = (const u16*)d_in[4];
  P.w2 = (const u16*)d_in[5];
  P.w3 = (const u16*)d_in[6];
  P.w4 = (const u16*)d_in[7];
  P.gw = (const u16*)d_in[8];
  P.ws = (char*)d_ws;
  P.out = (u16*)d_out;
  (void)hipFuncSetAttribute((const void*)mega_kernel, hipFuncAttributeMaxDynamicSharedMemorySize, LDS_BYTES);
  mega_kernel<<<256, 512, LDS_BYTES, stream>>>(P);
}
```

```cpp
#include <hip/hip_runtime.h>
#include <hip/hip_bf16.h>
#include <stdint.h>

typedef __attribute__((ext_vector_type(8))) __bf16 bf16x8;
typedef __attribute__((ext_vector_type(4))) __bf16 bf16x4;
typedef __attribute__((ext_vector_type(2))) __bf16 bf16x2;
typedef __attribute__((ext_vector_type(16))) float f32x16;
typedef __attribute__((ext_vector_type(4))) float f32x4;
typedef __attribute__((ext_vector_type(2))) float f32x2;
typedef __attribute__((ext_vector_type(4))) unsigned u32x4;
typedef __attribute__((ext_vector_type(2))) unsigned u32x2;
typedef unsigned short u16;
typedef unsigned long long u64;
#define LAS __attribute__((address_space(3)))
#define GAS __attribute__((address_space(1)))
#define DI __device__ __forceinline__
#define RLX_AGENT __ATOMIC_RELAXED, __HIP_MEMORY_SCOPE_AGENT

constexpr int S_ = 2048, D_ = 448, F_ = 1792;

constexpr unsigned WS_EPOCH = 0;
constexpr unsigned WS_FL = 256;
constexpr unsigned FL_P1 = 0, FL_X1 = 2048, FL_O = 3072, FL_H2 = 4096, FL_N = 6144;
constexpr unsigned WS_Q = 1u << 20;
constexpr unsigned WS_K = WS_Q + 16 * 64 * 2048;
constexpr unsigned WS_V = WS_K + 16 * 64 * 2048;
constexpr unsigned WS_H1 = WS_V + 16 * 64 * 2048;
constexpr unsigned WS_X1 = WS_H1 + 64 * 28 * 1024;
constexpr unsigned WS_ST = WS_X1 + 128 * 14 * 1024;
constexpr unsigned WS_O = WS_ST + 128 * 8 * 128;
constexpr unsigned WS_H2 = WS_O + 64 * 32 * 1024;
constexpr unsigned WS_G = WS_H2 + 64 * 112 * 1024;
constexpr unsigned WS_END = WS_G + 2048 * 4;

constexpr int L_W3 = 0;
constexpr int L_RED2 = 100352 + 256;
constexpr int L_CSF = L_RED2 + 49152;
constexpr int L_MISC = L_CSF + 7168;
constexpr int L_P1S = L_MISC + 256;
constexpr int LDS_BYTES = 160 * 1024;
constexpr unsigned CODE_BYTES = 46 * 1024;

#define STAMP(i)

#define MFMA32(a, b, c) __builtin_amdgcn_mfma_f32_32x32x16_bf16((a), (b), (c), 0, 0, 0)
#define MFMA16(a, b, c) __builtin_amdgcn_mfma_f32_16x16x32_bf16((a), (b), (c), 0, 0, 0)
#define WAIT_VM(n) asm volatile("s_waitcnt vmcnt(" #n ")" ::: "memory")
#define WAIT_LGKM0 asm volatile("s_waitcnt lgkmcnt(0)" ::: "memory")
#define BARRIER() do { asm volatile("s_waitcnt lgkmcnt(0)" ::: "memory"); __builtin_amdgcn_s_barrier(); asm volatile("" ::: "memory"); } while (0)

DI bf16x8 as_bf8(u32x4 v) { return __builtin_bit_cast(bf16x8, v); }
DI unsigned pk2(float lo, float hi) {
  bf16x2 r = {(__bf16)lo, (__bf16)hi};
  return __builtin_bit_cast(unsigned, r);
}
DI float bflo(unsigned u) { return __uint_as_float(u << 16); }
DI float bfhi(unsigned u) { return __uint_as_float(u & 0xffff0000u); }
DI float dot2(unsigned a, unsigned b, float c) {
  return __builtin_amdgcn_fdot2_f32_bf16(__builtin_bit_cast(bf16x2, a), __builtin_bit_cast(bf16x2, b), c, false);
}
DI float swap32_add(float v) {
  unsigned u = __float_as_uint(v);
  auto r = __builtin_amdgcn_permlane32_swap(u, u, false, false);
  return __uint_as_float(r[0]) + __uint_as_float(r[1]);
}
DI float swap16_add(float v) {
  unsigned u = __float_as_uint(v);
  auto r = __builtin_amdgcn_permlane16_swap(u, u, false, false);
  return __uint_as_float(r[0]) + __uint_as_float(r[1]);
}
DI float swap32_max(float v) {
  unsigned u = __float_as_uint(v);
  auto r = __builtin_amdgcn_permlane32_swap(u, u, false, false);
  return fmaxf(__uint_as_float(r[0]), __uint_as_float(r[1]));
}
DI float gelu_sig(float x) {
  const float c0 = -2.f * 0.7978845608028654f * 1.4426950408889634f;
  const float c1 = c0 * 0.044715f;
  float t = x * x;
  float p = __builtin_fmaf(t, c1, c0);
  float e = __builtin_amdgcn_exp2f(x * p);
  return __builtin_amdgcn_rcpf(1.f + e);
}
DI u32x4 pack8(const f32x16& a, int s) {
  u32x4 r;
  r[0] = pk2(a[8 * s + 0], a[8 * s + 1]);
  r[1] = pk2(a[8 * s + 2], a[8 * s + 3]);
  r[2] = pk2(a[8 * s + 4], a[8 * s + 5]);
  r[3] = pk2(a[8 * s + 6], a[8 * s + 7]);
  return r;
}
DI f32x16 splat16(float v) {
  f32x16 z;
#pragma unroll
  for (int i = 0; i < 16; ++i) z[i] = v;
  return z;
}
DI void dma16(const void* sbase, unsigned voff, unsigned ldst) {
  unsigned keep;
  asm volatile("s_mov_b32 %0, m0\n\ts_mov_b32 m0, %3\n\ts_nop 0\n\tglobal_load_lds_dwordx4 %1, %2\n\ts_mov_b32 m0, %0"
               : "=&s"(keep) : "v"(voff), "s"(sbase), "s"(ldst) : "memory");
}
DI bf16x8 tr_frag(const LAS char* p, int off2) {
  bf16x4 a = __builtin_amdgcn_ds_read_tr16_b64_v4bf16((LAS bf16x4*)(p));
  bf16x4 b = __builtin_amdgcn_ds_read_tr16_b64_v4bf16((LAS bf16x4*)(p + off2));
  return __builtin_shufflevector(a, b, 0, 1, 2, 3, 4, 5, 6, 7);
}
typedef __amdgpu_buffer_rsrc_t rsrc_t;
DI u32x4 ld16(rsrc_t rs, unsigned voff, unsigned soff) { return __builtin_amdgcn_raw_buffer_load_b128(rs, voff, soff, 16); }
DI u32x2 ld8(rsrc_t rs, unsigned voff, unsigned soff) { return __builtin_amdgcn_raw_buffer_load_b64(rs, voff, soff, 16); }
DI unsigned ld4(rsrc_t rs, unsigned voff, unsigned soff) { return __builtin_amdgcn_raw_buffer_load_b32(rs, voff, soff, 16); }
DI void st16(rsrc_t rs, u32x4 v, unsigned voff, unsigned soff) { __builtin_amdgcn_raw_buffer_store_b128(v, rs, voff + soff, 0, 16);    }
DI void st8(rsrc_t rs, u32x2 v, unsigned voff, unsigned soff) { __builtin_amdgcn_raw_buffer_store_b64(v, rs, voff, soff, 16); }
DI void st4(rsrc_t rs, unsigned v, unsigned voff, unsigned soff) { __builtin_amdgcn_raw_buffer_store_b32(v, rs, voff, soff, 16); }

DI void set_flag(char* ws, unsigned idx, u64 tag, int lane) {
  if (lane == 0) __hip_atomic_store((GAS u64*)(ws + WS_FL + (size_t)idx * 128), tag, RLX_AGENT);
}
DI void poll_flags(char* ws, int idx, u64 tag) {
  GAS u64* p = (GAS u64*)(ws + WS_FL + (size_t)max(idx, 0) * 128);
  for (int spins = 0; spins < (1 << 16); ++spins) {
    u64 v = __hip_atomic_load(p, RLX_AGENT);
    if (__all(idx < 0 || v == tag)) break;
    __builtin_amdgcn_s_sleep(1);
  }
  asm volatile("" ::: "memory");
}
DI void lds_arrive(LAS char* lds, int off, int lane) {
  WAIT_LGKM0;
  if (lane == 0) __hip_atomic_fetch_add((LAS unsigned*)(lds + off), 1u, __ATOMIC_RELAXED, __HIP_MEMORY_SCOPE_WORKGROUP);
}
DI void lds_wait(LAS char* lds, int off, unsigned n) {
  for (int spins = 0; spins < (1 << 20); ++spins) {
    unsigned v = __hip_atomic_load((LAS unsigned*)(lds + off), __ATOMIC_RELAXED, __HIP_MEMORY_SCOPE_WORKGROUP);
    if (__builtin_amdgcn_readfirstlane(v) >= n) break;
    __builtin_amdgcn_s_sleep(0);
  }
  asm volatile("" ::: "memory");
}
DI int fl_p1(int rt, int ct, int wv) { return FL_P1 + (rt * 16 + ct) * 8 + wv; }

struct Params {
  const u16 *x, *wqkv, *wo, *w1, *w2, *w3, *w4, *gw;
  char* ws;
  u16* out;
};

__global__ __launch_bounds__(512, 2) void mega_kernel(Params P) {
  extern __shared__ __attribute__((aligned(16))) char lds_raw[];
  LAS char* lds = (LAS char*)lds_raw;
  const unsigned lds0 = (unsigned)(size_t)lds;
  char* ws = P.ws;
  const int tid = threadIdx.x, lane = tid & 63, wave = __builtin_amdgcn_readfirstlane(tid >> 6);
  const int r = lane & 31, h = lane >> 5;
  const int b = blockIdx.x, xcd = b & 7, idx = b >> 3;
  const int rt = (xcd >> 1) * 4 + (idx & 3);
  const int ct = (xcd & 1) * 8 + (idx >> 2);
  const rsrc_t wsr = __builtin_amdgcn_make_buffer_rsrc(ws, 0, WS_END, 0x00020000);
  STAMP(0);
  unsigned dummy = 0;
  {
    u64 pc;
    asm volatile("s_getpc_b64 %0" : "=s"(pc));
    if (wave == 7) {
      const unsigned off = (unsigned)(idx * 2048 + lane * 32);
      if (off < CODE_BYTES) asm volatile("global_load_dword %0, %1, %2" : "+v"(dummy) : "v"(off), "s"(pc & ~127ull) : "memory");
    }
  }

  u64 tag;
  {
    const int type = ct >> 2;
    const int w4 = wave & 3, ch = wave >> 2;
    const int strip = rt * 4 + w4;
    int shift = 0;
    if (type == 3) { int g = ct & 3; shift = (g == 0) ? 1 : (g == 1) ? -1 : (g == 2) ? 2 : -2; }
    const int rot = (2 * (rt & 3) + ct) % 7;
    const u16* W = (type == 3) ? (P.w1 + (ct & 3) * 112) : (P.wqkv + ct * 112);
    const unsigned ldb = (type == 3) ? 896u : 2688u;
    unsigned xoff[2], woff[2];
    bool wact;
#pragma unroll
    for (int i = 0; i < 2; ++i) {
      int row = 16 * wave + 8 * i + (lane >> 3);
      int tok = min(max(rt * 128 + row - shift, 0), S_ - 1);
      xoff[i] = (unsigned)tok * 896u + (unsigned)(((lane & 7) ^ ((row >> 1) & 7)) * 16);
      int k = 8 * wave + 4 * i + (lane >> 4);
      int sg = (lane & 15) ^ ((k & 3) << 2);
      woff[i] = (unsigned)k * ldb + (unsigned)sg * 16u;
      wact = sg < 14;
    }
    u64 ep;
    unsigned gv[7];
    {
      asm volatile("global_load_dwordx2 %0, %1, %2 sc1" : "=v"(ep) : "v"(0u), "s"(ws + WS_EPOCH) : "memory");
#pragma unroll
      for (int c = 0; c < 7; ++c) {
        int cc = c + rot; cc = cc >= 7 ? cc - 7 : cc;
        asm volatile("global_load_ushort %0, %1, %2" : "=v"(gv[c]) : "v"((unsigned)(lane * 2)), "s"(P.gw + cc * 64) : "memory");
      }
    }
    auto issue = [&](int c) {
      int cc = c + rot; cc = cc >= 7 ? cc - 7 : cc;
      const char* xb = (const char*)P.x + cc * 128;
      const char* wb = (const char*)W + (size_t)cc * 64 * ldb;
      const unsigned sl = lds0 + (c & 3) * 32768;
      dma16(xb, xoff[0], sl + wave * 2048);
      dma16(xb, xoff[1], sl + wave * 2048 + 1024);
      if (wact) {
        dma16(wb, woff[0], sl + 16384 + wave * 2048);
        dma16(wb, woff[1], sl + 16384 + wave * 2048 + 1024);
      }
    };
    issue(0); issue(1); issue(2);
    if (tid < 16) *(LAS unsigned*)(lds + L_MISC + tid * 4) = 0u;
    if (tid < 128) *(LAS float*)(lds + L_P1S + tid * 4) = 0.f;

    const int blk = (lane >> 4) & 1, q = (lane & 15) >> 2, pp = lane & 3;
    const int ustride = (type == 3) ? 64 : 56;
    const LAS char* wa[2];
#pragma unroll
    for (int i = 0; i < 2; ++i) {
      int bo = ustride * (2 * ch + i) + 32 * blk + 8 * pp;
      wa[i] = lds + 16384 + (8 * h + q) * 256 + (((bo >> 6) ^ q) << 6) + (bo & 63);
    }
    const LAS char* xa[4];
#pragma unroll
    for (int s4 = 0; s4 < 4; ++s4) xa[s4] = lds + (w4 * 32 + r) * 128 + (((2 * s4 + h) ^ ((r >> 1) & 7)) * 16);
    const bool do_aux = (wave == 0) && (type == 3 || ct == 4);
    LAS char* auxp = lds + 16384 + lane * 256 + ((14 ^ ((lane & 3) << 2)) * 16);

    f32x16 acc[2], gram;
    acc[0] = splat16(0.f); acc[1] = splat16(0.f); gram = splat16(0.f);
    float cs0 = 0.f, cs1 = 0.f;

#pragma unroll
    for (int c = 0; c < 7; ++c) {
      if (c <= 4) { WAIT_VM(8); } else if (c == 5) { WAIT_VM(4); } else { WAIT_VM(0); }
      if (c == 0) asm volatile("" : "+v"(dummy), "+v"(ep), "+v"(gv[0]), "+v"(gv[1]), "+v"(gv[2]), "+v"(gv[3]), "+v"(gv[4]), "+v"(gv[5]), "+v"(gv[6]));
      if (do_aux) *(LAS u16*)(auxp + (c & 3) * 32768) = (type == 3) ? (u16)0x3F80 : (u16)gv[c];
      BARRIER();
      if (c == 0) STAMP(1);
      if (c + 3 < 7) issue(c + 3);
      const int so = (c & 3) * 32768;
#pragma unroll
      for (int s4 = 0; s4 < 4; ++s4) {
        u32x4 xf = *(const LAS u32x4*)(xa[s4] + so);
        bf16x8 wf0 = tr_frag(wa[0] + so + s4 * 4096, 1024);
        bf16x8 wf1 = tr_frag(wa[1] + so + s4 * 4096, 1024);
        if (type == 2) {
          acc[0] = MFMA32(as_bf8(xf), wf0, acc[0]);
          acc[1] = MFMA32(as_bf8(xf), wf1, acc[1]);
        } else {
          acc[0] = MFMA32(wf0, as_bf8(xf), acc[0]);
          acc[1] = MFMA32(wf1, as_bf8(xf), acc[1]);
        }
        if (type == 3) {
          if ((s4 >> 1) == ch) gram = MFMA32(as_bf8(xf), as_bf8(xf), gram);
          if (s4 == w4) {
            u32x4 a0 = __builtin_bit_cast(u32x4, wf0), a1 = __builtin_bit_cast(u32x4, wf1);
#pragma unroll
            for (int d = 0; d < 4; ++d) { cs0 = dot2(a0[d], 0x3F803F80u, cs0); cs1 = dot2(a1[d], 0x3F803F80u, cs1); }
          }
        }
      }
    }
    STAMP(2);
    tag = __builtin_amdgcn_readfirstlane((unsigned)ep) | ((u64)__builtin_amdgcn_readfirstlane((unsigned)(ep >> 32)) << 32);
    tag += 1;

    if (type == 2) {
#pragma unroll
      for (int i = 0; i < 2; ++i) {
        int head = (ct & 3) * 4 + 2 * ch + i;
#pragma unroll
        for (int s = 0; s < 2; ++s) {
          u32x4 v = pack8(acc[i], s);
          if (r >= 28) { unsigned cst = (r == 28) ? 0x3F803F80u : 0u; v[0] = cst; v[1] = cst; v[2] = cst; v[3] = cst; }
          st16(wsr, v, lane * 16, WS_V + ((head * 64 + strip) * 2 + s) * 1024);
        }
      }
      BARRIER();
    } else if (type < 2) {
      if (ct == 4 && ch == 1) {
        float glog = acc[1][12];
        float g = __builtin_amdgcn_rcpf(1.f + __builtin_amdgcn_exp2f(-1.4426950408889634f * glog));
        if (h == 1) st4(wsr, __float_as_uint(g), r * 4, WS_G + strip * 128);
      }
      const float sc = (type == 0) ? (0.1889822365046136f * 1.4426950408889634f) : 1.f;
      const unsigned dst = (type == 0 ? WS_Q : WS_K);
#pragma unroll
      for (int i = 0; i < 2; ++i) {
        int head = (ct & 3) * 4 + 2 * ch + i;
#pragma unroll
        for (int k = 0; k < 16; ++k) acc[i][k] *= sc;
#pragma unroll
        for (int s = 0; s < 2; ++s) {
          u32x4 v = pack8(acc[i], s);
          if (s == 1 && h == 1) { v[2] = 0u; v[3] = 0u; }
          st16(wsr, v, lane * 16, dst + ((head * 64 + strip) * 2 + s) * 1024);
        }
      }
      BARRIER();
    } else {
      LAS float* csl = (LAS float*)(lds + L_P1S);
      LAS float* sts = (LAS float*)(lds + L_P1S + 512);
      cs0 = swap32_add(cs0); cs1 = swap32_add(cs1);
      __hip_atomic_fetch_add(csl + (2 * ch + h) * 32 + r, h ? cs1 : cs0, __ATOMIC_RELAXED, __HIP_MEMORY_SCOPE_WORKGROUP);
      float dg = 0.f;
      const int dreg = (r & 3) + 4 * (r >> 3);
#pragma unroll
      for (int k = 0; k < 16; ++k) dg = (dreg == k) ? gram[k] : dg;
      if (h == ((r >> 2) & 1)) sts[(w4 * 3 + ch) * 32 + r] = dg;
      if (ch == 1 && h == 0) sts[(w4 * 3 + 2) * 32 + r] = acc[1][8];
      BARRIER();
      const float sxx = sts[(w4 * 3 + 0) * 32 + r] + sts[(w4 * 3 + 1) * 32 + r];
      const float sx = sts[(w4 * 3 + 2) * 32 + r];
      const float mean = sx * (1.f / 448.f);
      const float var = fmaxf(sxx * (1.f / 448.f) - mean * mean, 0.f);
      const float rstd = __builtin_amdgcn_rsqf(var + 1e-5f);
      u32x4 bm = {0u, 0u, 0u, 0u};
      {
        __bf16 mh = (__bf16)(-mean);
        float ml = -mean - (float)mh;
        bm[0] = h ? 0u : pk2((float)mh, (float)mh);
        bm[1] = h ? 0u : pk2(ml, 0.f);
      }
#pragma unroll
      for (int i = 0; i < 2; ++i) {
        float c = csl[(2 * ch + i) * 32 + r];
        __bf16 chh = (__bf16)c;
        float cl = c - (float)chh;
        u32x4 am = {0u, 0u, 0u, 0u};
        am[0] = h ? 0u : pk2((float)chh, cl);
        am[1] = h ? 0u : pk2((float)chh, 0.f);
        acc[i] = MFMA32(as_bf8(am), as_bf8(bm), acc[i]);
      }
      const int tok = strip * 32 + r - shift;
      const float rs = (tok >= 0 && tok < S_) ? rstd : 0.f;
      const unsigned dst = WS_H1 + ((strip * 4 + (ct & 3)) * 7 + 4 * ch) * 1024;
#pragma unroll
      for (int f = 0; f < 4; ++f) {
        if (f == 3 && ch == 1) break;
        const int i = f >> 1, s = f & 1;
        float y[8];
#pragma unroll
        for (int j = 0; j < 8; ++j) {
          float xv = acc[i][8 * s + j] * rs;
          y[j] = xv * gelu_sig(xv);
        }
        u32x4 v = {pk2(y[0], y[1]), pk2(y[2], y[3]), pk2(y[4], y[5]), pk2(y[6], y[7])};
        st16(wsr, v, lane * 16, dst + f * 1024);
      }
    }
    STAMP(3);
    if (wave < 7) {
#pragma unroll
      for (int t = 0; t < 7; ++t) {
        int p = 64 * t + lane;
        int pi = p / 14, c = p - pi * 14;
        int k = (pi & ~15) + 8 * ((pi >> 2) & 1) + 4 * ((pi >> 3) & 1) + (pi & 3);
        unsigned off = (unsigned)(k * 3584 + 16 * c);
#pragma unroll
        for (int gi = 0; gi < 2; ++gi) {
          int grp = wave * 2 + gi;
          dma16((const char*)(P.w3 + ct * 112) + (size_t)grp * 32 * 3584, off, lds0 + L_W3 + grp * 7168 + t * 1024);
        }
      }
      WAIT_VM(14);
    } else {
      WAIT_VM(0);
    }
    set_flag(ws, fl_p1(rt, ct, wave), tag, lane);
    STAMP(4);
  }

  const bool hasB = b < 224;
  const int mt = xcd * 4 + (idx & 3);
  const int nt = idx >> 2;

  if (wave < 4) {
    if (hasB) {
      const int g = wave;
      const u16* wp = P.w2 + (size_t)(112 * g + 4 * h) * D_ + 64 * nt + 2 * r;
      unsigned wr[7][8];
#pragma unroll
      for (int f = 0; f < 7; ++f)
#pragma unroll
        for (int j = 0; j < 8; ++j) wr[f][j] = *(const unsigned*)(wp + (size_t)(16 * f + 8 * (j >> 2) + (j & 3)) * D_);
      u32x4 xr[4];
      {
        const u16* xp = P.x + (size_t)(64 * mt + 32 * (g >> 1) + r) * D_ + 64 * nt + 8 * h;
#pragma unroll
        for (int a = 0; a < 4; ++a) xr[a] = *(const u32x4*)(xp + 16 * a);
      }
      poll_flags(ws, lane < 4 ? fl_p1(mt >> 1, 12 + g, ((2 * mt + (lane & 1)) & 3) + 4 * (lane >> 1)) : -1, tag);
      STAMP(5);
      lds_arrive(lds, L_MISC + 0, lane);
      u32x4 hf[2][7];
#pragma unroll
      for (int t = 0; t < 2; ++t)
#pragma unroll
        for (int f = 0; f < 7; ++f) hf[t][f] = ld16(wsr, lane * 16, WS_H1 + (((2 * mt + t) * 4 + g) * 7 + f) * 1024);
      f32x16 acc[2][2];
#pragma unroll
      for (int i = 0; i < 2; ++i)
#pragma unroll
        for (int t = 0; t < 2; ++t) acc[i][t] = splat16(0.f);
#pragma unroll
      for (int t = 0; t < 2; ++t)
#pragma unroll
        for (int f = 0; f < 7; ++f) {
          u32x4 a0, a1;
#pragma unroll
          for (int d = 0; d < 4; ++d) {
            a0[d] = __builtin_amdgcn_perm(wr[f][2 * d + 1], wr[f][2 * d], 0x05040100u);
            a1[d] = __builtin_amdgcn_perm(wr[f][2 * d + 1], wr[f][2 * d], 0x07060302u);
          }
          acc[0][t] = MFMA32(as_bf8(a0), as_bf8(hf[t][f]), acc[0][t]);
          acc[1][t] = MFMA32(as_bf8(a1), as_bf8(hf[t][f]), acc[1][t]);
        }
      STAMP(6);
      const int l32 = (r & 15) + 16 * h;
#pragma unroll
      for (int t = 0; t < 2; ++t) {
        const int o = 2 * t + (r >> 4);
        const int si = (g - o - 1) & 3;
        if (o != g) {
          LAS char* p = lds + L_RED2 + ((o * 3 + si) * 8) * 512 + l32 * 16;
#pragma unroll
          for (int i = 0; i < 2; ++i)
#pragma unroll
            for (int a = 0; a < 4; ++a) {
              const f32x16& A = acc[i][t];
              *(LAS f32x4*)(p + (i * 4 + a) * 512) = f32x4{A[4 * a], A[4 * a + 1], A[4 * a + 2], A[4 * a + 3]};
            }
        }
      }
      lds_arrive(lds, L_MISC + 4, lane);
      lds_wait(lds, L_MISC + 4, 4);
      {
        f32x16 o0 = (g >> 1) ? acc[0][1] : acc[0][0];
        f32x16 o1 = (g >> 1) ? acc[1][1] : acc[1][0];
        const LAS char* p = lds + L_RED2 + (g * 3 * 8) * 512 + l32 * 16;
#pragma unroll
        for (int si = 0; si < 3; ++si)
#pragma unroll
          for (int a = 0; a < 4; ++a) {
            f32x4 u0 = *(const LAS f32x4*)(p + (si * 8 + a) * 512);
            f32x4 u1 = *(const LAS f32x4*)(p + (si * 8 + 4 + a) * 512);
#pragma unroll
            for (int k = 0; k < 4; ++k) { o0[4 * a + k] += u0[k]; o1[4 * a + k] += u1[k]; }
          }
        float sx = 0.f, sxx = 0.f;
        const bool own = (r >> 4) == (g & 1);
        const int s16 = 4 * mt + g;
#pragma unroll
        for (int a = 0; a < 4; ++a) {
          u32x4 v;
#pragma unroll
          for (int k = 0; k < 4; ++k) {
            v[k] = pk2(o0[4 * a + k] + bflo(xr[a][k]), o1[4 * a + k] + bfhi(xr[a][k]));
            sx = dot2(v[k], 0x3F803F80u, sx);
            sxx = dot2(v[k], v[k], sxx);
          }
          if (own) st16(wsr, v, ((r & 15) + 16 * (2 * (a & 1) + h)) * 16, WS_X1 + (s16 * 14 + 2 * nt + (a >> 1)) * 1024);
        }
        sx = swap32_add(sx); sxx = swap32_add(sxx);
        if (own && h == 0) st8(wsr, u32x2{__float_as_uint(sx), __float_as_uint(sxx)}, (r & 15) * 8, WS_ST + (s16 * 8 + nt) * 128);
      }
      WAIT_VM(0);
      set_flag(ws, FL_X1 + (mt * 7 + nt) * 4 + g, tag, lane);
      STAMP(7);
    } else {
      WAIT_VM(0);
      lds_arrive(lds, L_MISC + 0, lane);
    }
    lds_wait(lds, L_MISC + 0, 8);
    {
      const int q4 = lane >> 4, q = (lane & 15) >> 2, pp = lane & 3;
      const LAS char* ab = lds + L_W3 + (16 * (q4 >> 1) + 4 * (q4 & 1) + q) * 224 + 8 * pp;
#pragma unroll
      for (int ti = 0; ti < 2; ++ti) {
        const int tau = wave + 4 * ti;
        if (tau < 7) {
          float c0 = 0.f, c1 = 0.f;
#pragma unroll
          for (int s = 0; s < 14; ++s) {
            u32x4 a = __builtin_bit_cast(u32x4, tr_frag(ab + s * 7168 + tau * 32, 1792));
            c0 = dot2(a[0], 0x3F803F80u, c0); c1 = dot2(a[1], 0x3F803F80u, c1);
            c0 = dot2(a[2], 0x3F803F80u, c0); c1 = dot2(a[3], 0x3F803F80u, c1);
          }
          float c = swap16_add(swap32_add(c0 + c1));
          __bf16 chh = (__bf16)c;
          float cl = c - (float)chh;
          u32x4 am = {0u, 0u, 0u, 0u};
          am[0] = q4 ? 0u : pk2((float)chh, cl);
          am[1] = q4 ? 0u : pk2((float)chh, 0.f);
          *(LAS u32x4*)(lds + L_CSF + tau * 1024 + lane * 16) = am;
        }
      }
    }
    STAMP(8);
  } else {
    const int head = 2 * xcd + (idx & 1), qs = (idx >> 1) * 4 + (wave - 4);
    {
      int fi = -1;
      const int hw = ((head & 3) >> 1) * 4;
      if (lane < 18) {
        int ks = qs - 8 + (lane % 9);
        if (ks >= 0) fi = fl_p1(ks >> 2, (lane < 9 ? 4 : 8) + (head >> 2), (ks & 3) + hw);
      } else if (lane == 18) fi = fl_p1(qs >> 2, head >> 2, (qs & 3) + hw);
      else if (lane == 19) fi = fl_p1(qs >> 2, 4, (qs & 3) + 4);
      poll_flags(ws, fi, tag);
    }
    STAMP(5);
    lds_arrive(lds, L_MISC + 0, lane);
    u32x4 qf0 = ld16(wsr, lane * 16, WS_Q + ((head * 64 + qs) * 2) * 1024);
    u32x4 qf1 = ld16(wsr, lane * 16, WS_Q + ((head * 64 + qs) * 2 + 1) * 1024);
    u32x4 kf[9][2], vf[9][2];
#pragma unroll
    for (int t = 0; t < 9; ++t) {
      int ks = max(qs - 8 + t, 0);
      kf[t][0] = ld16(wsr, lane * 16, WS_K + ((head * 64 + ks) * 2) * 1024);
      kf[t][1] = ld16(wsr, lane * 16, WS_K + ((head * 64 + ks) * 2 + 1) * 1024);
    }
#pragma unroll
    for (int t = 0; t < 9; ++t) {
      int ks = max(qs - 8 + t, 0);
      vf[t][0] = ld16(wsr, lane * 16, WS_V + ((head * 64 + ks) * 2) * 1024);
      vf[t][1] = ld16(wsr, lane * 16, WS_V + ((head * 64 + ks) * 2 + 1) * 1024);
    }
    const float gq = __uint_as_float(ld4(wsr, r * 4, WS_G + qs * 128));
    const float NEG = -1e30f;
    float m = NEG;
#pragma unroll
    for (int t = 0; t < 9; ++t) {
      if (qs - 8 + t >= 0) {
        f32x16 s = MFMA32(as_bf8(kf[t][0]), as_bf8(qf0), splat16(0.f));
        s = MFMA32(as_bf8(kf[t][1]), as_bf8(qf1), s);
#pragma unroll
        for (int i = 0; i < 16; ++i) {
          const int key = (i & 3) + 8 * (i >> 2) + 4 * h;
          float v = s[i];
          if (t == 0) v = (key > r) ? v : NEG;
          if (t == 8) v = (key <= r) ? v : NEG;
          m = fmaxf(m, v);
        }
      }
    }
    m = swap32_max(m);
    const f32x16 negm = splat16(-m);
    f32x16 o = splat16(0.f);
#pragma unroll
    for (int t = 0; t < 9; ++t) {
      if (qs - 8 + t >= 0) {
        f32x16 s = MFMA32(as_bf8(kf[t][0]), as_bf8(qf0), negm);
        s = MFMA32(as_bf8(kf[t][1]), as_bf8(qf1), s);
#pragma unroll
        for (int i = 0; i < 16; ++i) {
          const int key = (i & 3) + 8 * (i >> 2) + 4 * h;
          float v = __builtin_amdgcn_exp2f(s[i]);
          if (t == 0) v = (key > r) ? v : 0.f;
          if (t == 8) v = (key <= r) ? v : 0.f;
          s[i] = v;
        }
        o = MFMA32(as_bf8(vf[t][0]), as_bf8(pack8(s, 0)), o);
        o = MFMA32(as_bf8(vf[t][1]), as_bf8(pack8(s, 1)), o);
      }
    }
    float l = (h == 1) ? o[12] : 0.f;
    l = swap32_add(l);
    const float f = gq * __builtin_amdgcn_rcpf(l);
#pragma unroll
    for (int i = 0; i < 16; ++i) o[i] *= f;
    u32x4 o0 = pack8(o, 0), o1 = pack8(o, 1);
    if (h == 1) { o1[2] = 0u; o1[3] = 0u; }
    st16(wsr, o0, lane * 16, WS_O + ((qs * 16 + head) * 2) * 1024);
    st16(wsr, o1, lane * 16, WS_O + ((qs * 16 + head) * 2 + 1) * 1024);
    WAIT_VM(0);
    set_flag(ws, FL_O + qs * 16 + head, tag, lane);
    STAMP(7);
    lds_wait(lds, L_MISC + 0, 8);
    STAMP(8);
  }

  unsigned w4r[14][8];
  {
    const int s16 = rt * 8 + wave;
    const int t16 = lane & 15, q4 = lane >> 4, q = (lane & 15) >> 2, pp = lane & 3;
    poll_flags(ws, lane < 7 ? (int)(FL_X1 + ((s16 >> 2) * 7 + lane) * 4 + (s16 & 3)) : (lane == 7 ? fl_p1(rt, 4, (wave >> 1) + 4) : -1), tag);
    STAMP(9);
    u32x4 xf[14];
#pragma unroll
    for (int s = 0; s < 14; ++s) xf[s] = ld16(wsr, lane * 16, WS_X1 + (s16 * 14 + s) * 1024);
    u32x2 st0 = ld8(wsr, q4 * 128 + t16 * 8, WS_ST + s16 * 1024);
    u32x2 st1 = ld8(wsr, min(q4 + 4, 6) * 128 + t16 * 8, WS_ST + s16 * 1024);
    const float g = __uint_as_float(ld4(wsr, t16 * 4, WS_G + s16 * 64));
    const u16* wp0 = P.w4 + (size_t)(224 * wave + 4 * h) * D_ + 64 * min(nt, 6) + 2 * r;
    const u16* wp1 = P.w4 + (size_t)(224 * wave + 8 * h) * D_ + 64 * min(nt, 6) + 2 * r;
    const LAS char* ab = lds + L_W3 + (16 * (q4 >> 1) + 4 * (q4 & 1) + q) * 224 + 8 * pp;
    f32x4 acc[7];
#pragma unroll
    for (int tau = 0; tau < 7; ++tau) acc[tau] = f32x4{0.f, 0.f, 0.f, 0.f};
#pragma unroll
    for (int s = 0; s < 14; ++s) {
      {
        const int ks = s, c2 = ks / 7, kp = ks % 7;
#pragma unroll
        for (int j = 0; j < 8; ++j) {
          if (kp < 6) w4r[ks][j] = *(const unsigned*)(wp0 + (size_t)(112 * c2 + 32 * (kp >> 1) + 16 * (j >> 2) + 8 * (kp & 1) + (j & 3)) * D_);
          else w4r[ks][j] = *(const unsigned*)(wp1 + (size_t)(112 * c2 + 96 + j) * D_);
        }
      }
#pragma unroll
      for (int tau = 0; tau < 7; ++tau) {
        bf16x8 wf = tr_frag(ab + s * 7168 + tau * 32, 1792);
        acc[tau] = MFMA16(wf, as_bf8(xf[s]), acc[tau]);
      }
      __builtin_amdgcn_sched_barrier(0);
    }
    STAMP(10);
    float sx = __uint_as_float(st0[0]) + (q4 < 3 ? __uint_as_float(st1[0]) : 0.f);
    float sxx = __uint_as_float(st0[1]) + (q4 < 3 ? __uint_as_float(st1[1]) : 0.f);
    sx = swap16_add(swap32_add(sx));
    sxx = swap16_add(swap32_add(sxx));
    const float mean = sx * (1.f / 448.f);
    const float var = fmaxf(sxx * (1.f / 448.f) - mean * mean, 0.f);
    const float rstd = __builtin_amdgcn_rsqf(var + 1e-5f);
    u32x4 bm = {0u, 0u, 0u, 0u};
    {
      __bf16 mh = (__bf16)(-mean);
      float ml = -mean - (float)mh;
      bm[0] = q4 ? 0u : pk2((float)mh, (float)mh);
      bm[1] = q4 ? 0u : pk2(ml, 0.f);
    }
    BARRIER();
#pragma unroll
    for (int tau = 0; tau < 7; ++tau) {
      u32x4 am = *(const LAS u32x4*)(lds + L_CSF + tau * 1024 + lane * 16);
      acc[tau] = MFMA16(as_bf8(am), as_bf8(bm), acc[tau]);
    }
    const float rg = rstd * (1.f - g);
    unsigned yy[7][2];
#pragma unroll
    for (int tau = 0; tau < 7; ++tau) {
      float y[4];
#pragma unroll
      for (int k = 0; k < 4; ++k) {
        float a = acc[tau][k];
        y[k] = (a * rg) * gelu_sig(a * rstd);
      }
      yy[tau][0] = pk2(y[0], y[1]); yy[tau][1] = pk2(y[2], y[3]);
    }
    const unsigned dst = WS_H2 + (((s16 >> 1) * 16 + ct) * 7) * 1024;
    const int tr = 16 * (s16 & 1) + t16;
#pragma unroll
    for (int i = 0; i < 3; ++i) {
      u32x4 v = {yy[2 * i][0], yy[2 * i][1], yy[2 * i + 1][0], yy[2 * i + 1][1]};
      st16(wsr, v, (q4 >> 1) * 1024 + ((q4 & 1) * 32 + tr) * 16, dst + 2 * i * 1024);
    }
    {
      auto r0 = __builtin_amdgcn_permlane16_swap(yy[6][0], yy[6][0], false, false);
      auto r1 = __builtin_amdgcn_permlane16_swap(yy[6][1], yy[6][1], false, false);
      u32x4 v = {yy[6][0], yy[6][1], r0[1], r1[1]};
      if (!(q4 & 1)) st16(wsr, v, ((q4 >> 1) * 32 + tr) * 16, dst + 6 * 1024);
    }
    STAMP(11);
    WAIT_VM(0);
    set_flag(ws, FL_H2 + (rt * 16 + ct) * 8 + wave, tag, lane);
    STAMP(12);
  }

  if (hasB) {
    f32x16 acc[2][2];
#pragma unroll
    for (int i = 0; i < 2; ++i)
#pragma unroll
      for (int t = 0; t < 2; ++t) acc[i][t] = splat16(0.f);
    {
      const u16* wp = P.wo + 64 * nt + 2 * r;
      unsigned wr[4][8];
#pragma unroll
      for (int ks = 0; ks < 4; ++ks) {
        const int head = 2 * wave + (ks >> 1), s = ks & 1;
#pragma unroll
        for (int j = 0; j < 8; ++j) {
          int d = min(16 * s + 8 * (j >> 2) + 4 * h + (j & 3), 27);
          wr[ks][j] = *(const unsigned*)(wp + (size_t)(head * 28 + d) * D_);
        }
      }
      poll_flags(ws, lane < 4 ? (int)(FL_O + (2 * mt + (lane & 1)) * 16 + 2 * wave + (lane >> 1)) : -1, tag);
      u32x4 of[2][4];
#pragma unroll
      for (int t = 0; t < 2; ++t)
#pragma unroll
        for (int ks = 0; ks < 4; ++ks) of[t][ks] = ld16(wsr, lane * 16, WS_O + (((2 * mt + t) * 16 + 2 * wave) * 2 + ks) * 1024);
#pragma unroll
      for (int ks = 0; ks < 4; ++ks) {
        u32x4 a0, a1;
#pragma unroll
        for (int d = 0; d < 4; ++d) {
          a0[d] = __builtin_amdgcn_perm(wr[ks][2 * d + 1], wr[ks][2 * d], 0x05040100u);
          a1[d] = __builtin_amdgcn_perm(wr[ks][2 * d + 1], wr[ks][2 * d], 0x07060302u);
        }
#pragma unroll
        for (int t = 0; t < 2; ++t) {
          acc[0][t] = MFMA32(as_bf8(a0), as_bf8(of[t][ks]), acc[0][t]);
          acc[1][t] = MFMA32(as_bf8(a1), as_bf8(of[t][ks]), acc[1][t]);
        }
      }
    }
    u32x4 wa[14][2];
#pragma unroll
    for (int ks = 0; ks < 14; ++ks)
#pragma unroll
      for (int d = 0; d < 4; ++d) {
        wa[ks][0][d] = __builtin_amdgcn_perm(w4r[ks][2 * d + 1], w4r[ks][2 * d], 0x05040100u);
        wa[ks][1][d] = __builtin_amdgcn_perm(w4r[ks][2 * d + 1], w4r[ks][2 * d], 0x07060302u);
      }
    const int ts = wave >> 2, a_own = wave & 3;
    const int tok = 64 * mt + 32 * ts + r;
    u32x4 x1v = ld16(wsr, ((r & 15) + 16 * (2 * (a_own & 1) + h)) * 16 + (r >> 4) * 14 * 1024,
                     WS_X1 + ((4 * mt + 2 * ts) * 14 + 2 * nt + (a_own >> 1)) * 1024);
    const float og = 1.f - __uint_as_float(ld4(wsr, r * 4, WS_G + (2 * mt + ts) * 128));
    STAMP(13);
    poll_flags(ws, lane < 8 ? (int)(FL_H2 + ((mt >> 1) * 16 + 2 * wave + (lane & 1)) * 8 + 4 * (mt & 1) + (lane >> 1)) : -1, tag);
    STAMP(14);
    constexpr int WIN = 11;
    u32x4 hf[28];
    const unsigned h2base = WS_H2 + (((2 * mt) * 16 + 2 * wave) * 7) * 1024;
#pragma unroll
    for (int i = 0; i < WIN; ++i) hf[i] = ld16(wsr, lane * 16, h2base + (i / 14) * 112 * 1024 + (i % 14) * 1024);
#pragma unroll
    for (int t = 0; t < 2; ++t) {
#pragma unroll
      for (int ks = 0; ks < 14; ++ks) {
        acc[0][t] = MFMA32(as_bf8(wa[ks][0]), as_bf8(hf[14 * t + ks]), acc[0][t]);
        acc[1][t] = MFMA32(as_bf8(wa[ks][1]), as_bf8(hf[14 * t + ks]), acc[1][t]);
        const int i = 14 * t + ks + WIN;
        if (i < 28) {
          hf[i] = ld16(wsr, lane * 16, h2base + (i / 14) * 112 * 1024 + (i % 14) * 1024);
          __builtin_amdgcn_sched_barrier(0);
        }
      }
#pragma unroll
      for (int a = 0; a < 4; ++a) {
        const int o = 4 * t + a;
        if (o != wave) {
          const int si = (wave - o - 1) & 7;
#pragma unroll
          for (int i = 0; i < 2; ++i) {
            const f32x16& A = acc[i][t];
            *(LAS f32x4*)(lds + ((o * 7 + si) * 2 + i) * 1024 + lane * 16) = f32x4{A[4 * a], A[4 * a + 1], A[4 * a + 2], A[4 * a + 3]};
          }
        }
      }
    }
    STAMP(15);
    BARRIER();
    f32x4 fin[2];
#pragma unroll
    for (int i = 0; i < 2; ++i) {
      f32x16 A = ts ? acc[i][1] : acc[i][0];
      f32x4 own;
      if (a_own == 0) own = f32x4{A[0], A[1], A[2], A[3]};
      else if (a_own == 1) own = f32x4{A[4], A[5], A[6], A[7]};
      else if (a_own == 2) own = f32x4{A[8], A[9], A[10], A[11]};
      else own = f32x4{A[12], A[13], A[14], A[15]};
#pragma unroll
      for (int si = 0; si < 7; ++si) own += *(const LAS f32x4*)(lds + ((wave * 7 + si) * 2 + i) * 1024 + lane * 16);
      fin[i] = own;
    }
    u32x4 o;
#pragma unroll
    for (int k = 0; k < 4; ++k)
      o[k] = pk2(__builtin_fmaf(og, bflo(x1v[k]), fin[0][k]), __builtin_fmaf(og, bfhi(x1v[k]), fin[1][k]));
    *(u32x4*)(P.out + (size_t)tok * D_ + 64 * nt + 16 * a_own + 8 * h) = o;
    STAMP(16);
  } else if (b == 255 && wave == 0) {
#pragma unroll 1
    for (int i = 0; i < 4; ++i) poll_flags(ws, FL_P1 + (i * 64 + lane) * 8, tag);
    if (lane == 0) __hip_atomic_store((GAS u64*)(ws + WS_EPOCH), tag, RLX_AGENT);
  }
}

extern "C" void kernel_launch(void* const* d_in, const int* in_sizes, int n_in, void* d_out, int out_size, void* d_ws,
                              size_t ws_size, hipStream_t stream) {
  Params P;
  P.x = (const u16*)d_in[0];
  P.wqkv = (const u16*)d_in[1];
  P.wo = (const u16*)d_in[2];
  P.w1 = (const u16*)d_in[4];
  P.w2 = (const u16*)d_in[5];
  P.w3 = (const u16*)d_in[6];
  P.w4 = (const u16*)d_in[7];
  P.gw = (const u16*)d_in[8];
  P.ws = (char*)d_ws;
  P.out = (u16*)d_out;
  (void)hipFuncSetAttribute((const void*)mega_kernel, hipFuncAttributeMaxDynamicSharedMemorySize, LDS_BYTES);
  mega_kernel<<<256, 512, LDS_BYTES, stream>>>(P);
}
```

```cpp
#include <hip/hip_runtime.h>
#include <hip/hip_bf16.h>
#include <stdint.h>

typedef __attribute__((ext_vector_type(8))) __bf16 bf16x8;
typedef __attribute__((ext_vector_type(4))) __bf16 bf16x4;
typedef __attribute__((ext_vector_type(2))) __bf16 bf16x2;
typedef __attribute__((ext_vector_type(16))) float f32x16;
typedef __attribute__((ext_vector_type(4))) float f32x4;
typedef __attribute__((ext_vector_type(2))) float f32x2;
typedef __attribute__((ext_vector_type(4))) unsigned u32x4;
typedef __attribute__((ext_vector_type(2))) unsigned u32x2;
typedef unsigned short u16;
typedef unsigned long long u64;
#define LAS __attribute__((address_space(3)))
#define GAS __attribute__((address_space(1)))
#define DI __device__ __forceinline__
#define RLX_AGENT __ATOMIC_RELAXED, __HIP_MEMORY_SCOPE_AGENT

constexpr int S_ = 2048, D_ = 448, F_ = 1792;

constexpr unsigned WS_EPOCH = 0;
constexpr unsigned WS_FL = 256;
constexpr unsigned FL_P1 = 0, FL_X1 = 2048, FL_O = 3072, FL_H2 = 4096, FL_SV = 6144, FL_N = 6400;
constexpr unsigned WS_Q = 1u << 20;
constexpr unsigned WS_K = WS_Q + 16 * 64 * 2048;
constexpr unsigned WS_V = WS_K + 16 * 64 * 2048;
constexpr unsigned WS_H1 = WS_V + 16 * 64 * 2048;
constexpr unsigned WS_X1 = WS_H1 + 128 * 14 * 1024;
constexpr unsigned WS_ST = WS_X1 + 128 * 14 * 1024;
constexpr unsigned WS_O = WS_ST + 128 * 8 * 128;
constexpr unsigned WS_H2 = WS_O + 64 * 32 * 1024;
constexpr unsigned WS_G = WS_H2 + 64 * 112 * 1024;
constexpr unsigned WS_W4T = WS_G + 2048 * 4;
constexpr unsigned WS_WOT = WS_W4T + 7 * 8 * 28 * 1024;
constexpr unsigned WS_CS = WS_WOT + 7 * 8 * 8 * 1024;
constexpr unsigned WS_END = WS_CS + 1792 * 4;

constexpr int L_W3 = 0;
constexpr int L_R2 = 100608;
constexpr int L_MISC = L_R2 + 57344;
constexpr int L_P1S = L_MISC + 512;
constexpr int L_CS3 = L_P1S + 2048;
constexpr int L_SINK = L_CS3 + 512;
constexpr int L_TAG = L_SINK + 256;
constexpr int LDS_BYTES = 160 * 1024;
#define CODE_BYTES_V $
constexpr unsigned CODE_BYTES = CODE_BYTES_V;

#define STAMP(i)

#define MFMA32(a, b, c) __builtin_amdgcn_mfma_f32_32x32x16_bf16((a), (b), (c), 0, 0, 0)
#define MFMA16(a, b, c) __builtin_amdgcn_mfma_f32_16x16x32_bf16((a), (b), (c), 0, 0, 0)
#define WAIT_VM(n) asm volatile("s_waitcnt vmcnt(" #n ")" ::: "memory")
#define WAIT_LGKM0 asm volatile("s_waitcnt lgkmcnt(0)" ::: "memory")
#define BARRIER() do { asm volatile("s_waitcnt lgkmcnt(0)" ::: "memory"); __builtin_amdgcn_s_barrier(); asm volatile("" ::: "memory"); } while (0)

DI bf16x8 as_bf8(u32x4 v) { return __builtin_bit_cast(bf16x8, v); }
DI unsigned pk2(float lo, float hi) {
  bf16x2 r = {(__bf16)lo, (__bf16)hi};
  return __builtin_bit_cast(unsigned, r);
}
DI float bflo(unsigned u) { return __uint_as_float(u << 16); }
DI float bfhi(unsigned u) { return __uint_as_float(u & 0xffff0000u); }
DI float dot2(unsigned a, unsigned b, float c) {
  return __builtin_amdgcn_fdot2_f32_bf16(__builtin_bit_cast(bf16x2, a), __builtin_bit_cast(bf16x2, b), c, false);
}
DI float swap32_add(float v) {
  unsigned u = __float_as_uint(v);
  auto r = __builtin_amdgcn_permlane32_swap(u, u, false, false);
  return __uint_as_float(r[0]) + __uint_as_float(r[1]);
}
DI float swap16_add(float v) {
  unsigned u = __float_as_uint(v);
  auto r = __builtin_amdgcn_permlane16_swap(u, u, false, false);
  return __uint_as_float(r[0]) + __uint_as_float(r[1]);
}
DI float swap32_max(float v) {
  unsigned u = __float_as_uint(v);
  auto r = __builtin_amdgcn_permlane32_swap(u, u, false, false);
  return fmaxf(__uint_as_float(r[0]), __uint_as_float(r[1]));
}
DI float max3f(float a, float b, float c) { return __builtin_fmaxf(__builtin_fmaxf(a, b), c); }
DI float gelu_sig(float x) {
  const float c0 = -2.f * 0.7978845608028654f * 1.4426950408889634f;
  const float c1 = c0 * 0.044715f;
  float t = x * x;
  float p = __builtin_fmaf(t, c1, c0);
  float e = __builtin_amdgcn_exp2f(x * p);
  return __builtin_amdgcn_rcpf(1.f + e);
}
DI unsigned gelu2(f32x2 a, float si, float so) {
  const float c0 = -2.f * 0.7978845608028654f * 1.4426950408889634f;
  const float c1 = c0 * 0.044715f;
  f32x2 x = a * si;
  f32x2 t = x * x;
  f32x2 p = __builtin_elementwise_fma(t, f32x2{c1, c1}, f32x2{c0, c0});
  f32x2 z = x * p;
  f32x2 e = {__builtin_amdgcn_exp2f(z[0]), __builtin_amdgcn_exp2f(z[1])};
  f32x2 d = e + 1.f;
  f32x2 rr = {__builtin_amdgcn_rcpf(d[0]), __builtin_amdgcn_rcpf(d[1])};
  f32x2 y = (a * so) * rr;
  return pk2(y[0], y[1]);
}
DI u32x4 pack8(const f32x16& a, int s) {
  u32x4 r;
  r[0] = pk2(a[8 * s + 0], a[8 * s + 1]);
  r[1] = pk2(a[8 * s + 2], a[8 * s + 3]);
  r[2] = pk2(a[8 * s + 4], a[8 * s + 5]);
  r[3] = pk2(a[8 * s + 6], a[8 * s + 7]);
  return r;
}
DI f32x16 splat16(float v) {
  f32x16 z;
#pragma unroll
  for (int i = 0; i < 16; ++i) z[i] = v;
  return z;
}
DI void dma16(const void* sbase, unsigned voff, unsigned ldst) {
  asm volatile("s_mov_b32 m0, %2\n\ts_nop 0\n\tglobal_load_lds_dwordx4 %0, %1" :: "v"(voff), "s"(sbase), "s"(ldst) : "memory");
}
DI void dma4(const void* sbase, unsigned voff, unsigned ldst) {
  asm volatile("s_mov_b32 m0, %2\n\ts_nop 0\n\tglobal_load_lds_dword %0, %1" :: "v"(voff), "s"(sbase), "s"(ldst) : "memory");
}
DI bf16x8 tr_frag(const LAS char* p, int off2) {
  bf16x4 a = __builtin_amdgcn_ds_read_tr16_b64_v4bf16((LAS bf16x4*)(p));
  bf16x4 b = __builtin_amdgcn_ds_read_tr16_b64_v4bf16((LAS bf16x4*)(p + off2));
  return __builtin_shufflevector(a, b, 0, 1, 2, 3, 4, 5, 6, 7);
}
typedef __amdgpu_buffer_rsrc_t rsrc_t;
DI u32x4 ld16(rsrc_t rs, unsigned voff, unsigned soff) { return __builtin_amdgcn_raw_buffer_load_b128(rs, voff, soff, 16); }
DI u32x2 ld8(rsrc_t rs, unsigned voff, unsigned soff) { return __builtin_amdgcn_raw_buffer_load_b64(rs, voff, soff, 16); }
DI unsigned ld4(rsrc_t rs, unsigned voff, unsigned soff) { return __builtin_amdgcn_raw_buffer_load_b32(rs, voff, soff, 16); }
DI void st16(rsrc_t rs, u32x4 v, unsigned voff, unsigned soff) { __builtin_amdgcn_raw_buffer_store_b128(v, rs, voff + soff, 0, 16);    }
DI void st8(rsrc_t rs, u32x2 v, unsigned voff, unsigned soff) { __builtin_amdgcn_raw_buffer_store_b64(v, rs, voff, soff, 16); }
DI void st4(rsrc_t rs, unsigned v, unsigned voff, unsigned soff) { __builtin_amdgcn_raw_buffer_store_b32(v, rs, voff, soff, 16); }

DI void set_flag(char* ws, unsigned idx, u64 tag, int lane) {
  if (lane == 0) __hip_atomic_store((GAS u64*)(ws + WS_FL + (size_t)idx * 128), tag, RLX_AGENT);
}
DI void poll_flags(char* ws, int idx, u64 tag) {
  GAS u64* p = (GAS u64*)(ws + WS_FL + (size_t)max(idx, 0) * 128);
#pragma unroll 1
  for (int spins = 0; spins < (1 << 16); ++spins) {
    u64 v = tag;
    if (idx >= 0) v = __hip_atomic_load(p, RLX_AGENT);
    if (__all(v == tag)) break;
    __builtin_amdgcn_s_sleep(4);
  }
  asm volatile("" ::: "memory");
}
DI void poll_flags_pub(char* ws, int idx, u64 tag, int pub, int lane) {
  GAS u64* p = (GAS u64*)(ws + WS_FL + (size_t)max(idx, 0) * 128);
#pragma unroll 1
  for (int spins = 0; spins < (1 << 16); ++spins) {
    u64 v = tag;
    if (idx >= 0) v = __hip_atomic_load(p, RLX_AGENT);
    WAIT_VM(0);
    if (pub >= 0) { set_flag(ws, pub, tag, lane); pub = -1; }
    if (__all(v == tag)) break;
    __builtin_amdgcn_s_sleep(4);
  }
  asm volatile("" ::: "memory");
}
DI void lds_arrive(LAS char* lds, int off, int lane) {
  WAIT_LGKM0;
  if (lane == 0) __hip_atomic_fetch_add((LAS unsigned*)(lds + off), 1u, __ATOMIC_RELAXED, __HIP_MEMORY_SCOPE_WORKGROUP);
}
DI void lds_wait(LAS char* lds, int off, unsigned n) {
#pragma unroll 1
  for (int spins = 0; spins < (1 << 20); ++spins) {
    unsigned v = __hip_atomic_load((LAS unsigned*)(lds + off), __ATOMIC_RELAXED, __HIP_MEMORY_SCOPE_WORKGROUP);
    if (__builtin_amdgcn_readfirstlane(v) >= n) break;
    __builtin_amdgcn_s_sleep(1);
  }
  asm volatile("" ::: "memory");
}
DI u64 read_tag(LAS char* lds) {
  u32x2 t = *(volatile LAS u32x2*)(lds + L_TAG);
  return (u64)__builtin_amdgcn_readfirstlane(t[0]) | ((u64)__builtin_amdgcn_readfirstlane(t[1]) << 32);
}
DI int fl_p1(int rt, int ct, int wv) { return FL_P1 + (rt * 16 + ct) * 8 + wv; }

#define TOUCH_AT 5
struct Params {
  const u16 *x, *wqkv, *wo, *w1, *w2, *w3, *w4, *gw;
  char* ws;
  u16* out;
};
#define SB0 __builtin_amdgcn_sched_barrier(0)

DI u32x4 lds16(unsigned a) { return *(const LAS u32x4*)(size_t)a; }
DI bf16x8 trf(unsigned a, int off2) { return tr_frag((const LAS char*)(size_t)a, off2); }

#define TICK(v)
template <int TYPE>
DI void p1_consume(const unsigned (&xa)[4], const unsigned (&wa)[4], f32x16 (&acc)[4], f32x16& gram) {
  u32x4 xf[2];
  bf16x8 wf[2][4];
  BARRIER();
#pragma unroll
  for (int i = 0; i < 2; ++i) {
    xf[i] = lds16(xa[i]);
#pragma unroll
    for (int u = 0; u < 4; ++u) wf[i][u] = trf(wa[u] + i * 4096, 1024);
  }
  unsigned wb[4], xb[4];
#pragma unroll
  for (int u = 0; u < 4; ++u) { wb[u] = wa[u]; xb[u] = xa[u]; }
#pragma unroll 1
  for (int c = 0; c < 7; ++c) {
#pragma unroll
    for (int i = 0; i < 4; ++i) {
      if (i == 2) {
        const unsigned sn = ((c + 1) & 3) * 32768;
#pragma unroll
        for (int u = 0; u < 4; ++u) { wb[u] = wa[u] + sn; xb[u] = xa[u] + sn; }
        asm volatile("" : "+v"(wb[0]), "+v"(wb[1]), "+v"(wb[2]), "+v"(wb[3]), "+v"(xb[0]), "+v"(xb[1]), "+v"(xb[2]), "+v"(xb[3]));
        if (c < 6) BARRIER();
      }
      const int ni = (i + 2) & 3;
      const u32x4 xc = xf[i & 1];
      SB0;
#pragma unroll
      for (int u = 0; u < 4; ++u) {
        if (TYPE == 2) acc[u] = MFMA32(as_bf8(xc), wf[i & 1][u], acc[u]);
        else acc[u] = MFMA32(wf[i & 1][u], as_bf8(xc), acc[u]);
        SB0;
        wf[i & 1][u] = trf(wb[u] + ni * 4096, 1024);
        SB0;
      }
      if (TYPE == 3) { gram = MFMA32(as_bf8(xc), as_bf8(xc), gram); SB0; }
      xf[i & 1] = lds16(xb[ni]);
      SB0;
    }
  }
}

DI void w4_issue(const char* src, unsigned soff, unsigned dst) {
#pragma unroll
  for (int j = 0; j < 14; ++j) dma16(src + j * 8 * 896, soff, dst + j * 1024);
}
DI void w4_read(bf16x8 (&wa)[14][2], int k0, unsigned stg, const unsigned (&lb)[2], int h) {
#pragma unroll
  for (int kp = 0; kp < 7; ++kp)
#pragma unroll
    for (int i = 0; i < 2; ++i) {
      if (kp < 6) wa[k0 + kp][i] = trf(stg + lb[i] + (32 * (kp >> 1) + 8 * (kp & 1)) * 128, 16 * 128);
      else wa[k0 + kp][i] = trf(stg + lb[i] + 4 * h * 128 + 96 * 128, 4 * 128);
    }
}

__global__ __launch_bounds__(512, 2) void mega_kernel(Params P) {
  u64 pc0;
  asm volatile("s_getpc_b64 %0" : "=s"(pc0));
  extern __shared__ __attribute__((aligned(16))) char lds_raw[];
  LAS char* lds = (LAS char*)lds_raw;
  const unsigned lds0 = (unsigned)(size_t)lds;
  char* ws = P.ws;
  const int tid = threadIdx.x, lane = tid & 63, wave = __builtin_amdgcn_readfirstlane(tid >> 6);
  const int r = lane & 31, h = lane >> 5;
  const int b = blockIdx.x, xcd = b & 7, idx = b >> 3;
  const int rt = (xcd >> 1) * 4 + (idx & 3);
  const int ct = (xcd & 1) * 8 + (idx >> 2);
  const bool hasB = idx >= 4;
  const int mt = xcd * 4 + (idx & 3);
  const int nt = max((idx >> 2) - 1, 0);
  const rsrc_t wsr = __builtin_amdgcn_make_buffer_rsrc(ws, 0, WS_END, 0x00020000);
  STAMP(0);

  const int t16 = lane & 15, q4 = lane >> 4, q = (lane & 15) >> 2, pp = lane & 3;
  const int c1 = 2 * (idx >> 2) + (xcd & 1);
  u64 tag;
  const int type = c1 >> 2;
  int shift = 0;
  if (type == 3) { int g = c1 & 3; shift = (g & 2) ? 2 : 1; shift = (g & 1) ? -shift : shift; }
  if (__builtin_expect(wave >= 4, 1)) {
    const int p = wave - 4;
#define ROTX ((2 * (rt & 3) + (c1 >> 1)) % 7)
    const int rot = ROTX;
    if (__builtin_expect(p < 2, 1)) {
      const char* xsrc = (const char*)P.x;
      const char* wsrc = (const char*)((type == 3) ? (P.w1 + (c1 & 3) * 112) : (P.wqkv + c1 * 112));
      const unsigned ldb = (type == 3) ? 896u : 2688u;
      unsigned xoff[8], woff[8];
      const int sg = (lane & 15) ^ ((lane >> 4) << 2);
#pragma unroll
      for (int i = 0; i < 8; ++i) {
        int row = 64 * p + 8 * i + (lane >> 3);
        int tok = min(max(rt * 128 + row - shift, 0), S_ - 1);
        xoff[i] = (unsigned)tok * 896u + (unsigned)(((lane & 7) ^ ((row >> 1) & 7)) * 16);
        woff[i] = (unsigned)(32 * p + 4 * i + (lane >> 4)) * ldb + (unsigned)sg * 16u;
      }
      const bool wact = sg < 14;
      auto issue = [&](int c) {
        int cc = c + rot; cc = cc >= 7 ? cc - 7 : cc;
        const char* xb = xsrc + cc * 128;
        const char* wb = wsrc + (unsigned)cc * 64u * ldb;
        const unsigned sl = lds0 + (c & 3) * 32768 + p * 8192;
#pragma unroll
        for (int i = 0; i < 8; ++i) dma16(xb, xoff[i], sl + i * 1024);
        if (wact) {
#pragma unroll
          for (int i = 0; i < 8; ++i) dma16(wb, woff[i], sl + 16384 + i * 1024);
        }
      };
      STAMP(20);
      issue(0);
      STAMP(21);
#pragma unroll 1
      for (int c = 0; c < 6; ++c) {
        issue(c + 1);
        WAIT_VM(16);
        __builtin_amdgcn_s_barrier();
        if (c == 0) STAMP(1);
      }
      STAMP(22);
      WAIT_VM(0);
      __builtin_amdgcn_s_barrier();
    } else {
      const int sp = p - 2;
      if (type == 3) {
        f32x16 cs[4];
        unsigned swa[4];
        const int blk = (lane >> 4) & 1;
        const u32x4 ones = {0x3F803F80u, 0x3F803F80u, 0x3F803F80u, 0x3F803F80u};
#pragma unroll
        for (int i = 0; i < 4; ++i) { cs[i] = splat16(0.f); swa[i] = lds0 + 16384 + sp * 8192 + (8 * h + q) * 256 + ((i ^ q) << 6) + 32 * blk + 8 * pp; }
        __builtin_amdgcn_s_setprio(3);
#pragma unroll 1
        for (int c = 0; c < 7; ++c) {
          BARRIER();
          const unsigned so = (c & 3) * 32768;
#pragma unroll
          for (int t = 0; t < 2; ++t)
#pragma unroll
            for (int i = 0; i < 4; ++i) cs[i] = MFMA32(as_bf8(ones), trf(swa[i] + so + t * 4096, 1024), cs[i]);
        }
        LAS float* csl = (LAS float*)(lds + L_P1S);
        __hip_atomic_fetch_add(csl + (2 * h) * 32 + r, h ? cs[2][0] : cs[0][0], __ATOMIC_RELAXED, __HIP_MEMORY_SCOPE_WORKGROUP);
        __hip_atomic_fetch_add(csl + (2 * h + 1) * 32 + r, h ? cs[3][0] : cs[1][0], __ATOMIC_RELAXED, __HIP_MEMORY_SCOPE_WORKGROUP);
        lds_arrive(lds, L_MISC + 16, lane);
        lds_arrive(lds, L_MISC + 12, lane);
        __builtin_amdgcn_s_setprio(0);
      } else if (sp == 0) {
        unsigned lb[2];
#pragma unroll
        for (int i = 0; i < 2; ++i) lb[i] = q * 128 + ((i ^ (q >> 1)) * 64) + 32 * ((lane >> 4) & 1) + 8 * pp + 4 * h * 128;
        const unsigned soff = (unsigned)((lane >> 3) * 896 + (((lane & 7) ^ (((lane >> 4) & 1) << 2)) * 16));
const int job = rt * 12 + c1;
        const bool act = job < 140, is4 = job < 112;
        const int jn = is4 ? (job >> 4) : ((job - 112) >> 2), ju = is4 ? (job & 15) : ((job - 112) & 3);
        const unsigned stg = lds0 + 131072;
        BARRIER();
        if (act) w4_issue((const char*)((is4 ? P.w4 : P.wo) + 64 * jn) + (unsigned)(ju * 112 * 896), soff, stg);
        BARRIER();
        BARRIER();
        WAIT_VM(0);
        if (act) {
          if (is4) {
            bf16x8 fr[14][2];
            w4_read(fr, 0, stg, lb, h);
#pragma unroll
            for (int k = 0; k < 14; ++k)
              st16(wsr, __builtin_bit_cast(u32x4, fr[k >> 1][k & 1]), lane * 16, WS_W4T + ((jn * 16 + ju) * 14 + k) * 1024);
          } else {
#pragma unroll
            for (int k = 0; k < 16; ++k) {
              const int s_ = (k >> 1) & 1;
              const unsigned ro = stg + lb[k & 1] + (28 * (k >> 2) + 16 * s_) * 128;
              const int e1 = s_ ? (8 * 128 - 4 * h * 128) : 8 * 128;
              bf16x4 a0 = __builtin_amdgcn_ds_read_tr16_b64_v4bf16((LAS bf16x4*)(size_t)(ro));
              bf16x4 a1 = __builtin_amdgcn_ds_read_tr16_b64_v4bf16((LAS bf16x4*)(size_t)(ro + e1));
              st16(wsr, __builtin_bit_cast(u32x4, __builtin_shufflevector(a0, a1, 0, 1, 2, 3, 4, 5, 6, 7)), lane * 16,
                   WS_WOT + ((jn * 4 + ju) * 16 + k) * 1024);
            }
          }
        }
        BARRIER();
        BARRIER();
        BARRIER();
        WAIT_VM(0);
        if (act) set_flag(ws, FL_SV + job, read_tag(lds), lane);
        BARRIER();
        lds_arrive(lds, L_MISC + 12, lane);
      } else {
        const bool gatew = (c1 == 4);
        unsigned gv[7];
#pragma unroll
        for (int c = 0; c < 7; ++c) {
          int cc = c + rot; cc = cc >= 7 ? cc - 7 : cc;
          gv[c] = P.gw[cc * 64 + lane];
        }
        const unsigned auxa = lds0 + 16384 + lane * 256 + ((14 ^ ((lane & 3) << 2)) * 16);
        asm volatile("" : "+v"(gv[0]), "+v"(gv[1]), "+v"(gv[2]), "+v"(gv[3]), "+v"(gv[4]), "+v"(gv[5]), "+v"(gv[6]));
        WAIT_VM(0);
        auto touch = [&](int i) {
          if (i < 147) {
            const char* base; unsigned off;
            if (i < 49) { base = (const char*)P.w2; off = (unsigned)(i * 64 + lane) * 128u; }
            else { int l = (i - 49) * 64 + lane; int row = (l * 9363) >> 17; base = (const char*)P.w3 + (xcd & 1) * 1792; off = (unsigned)(row * 3584 + (l - row * 14) * 128); }
            dma4(base, off, lds0 + L_SINK);
          }
        };
#pragma unroll 1
        for (int c = 0; c < 7; ++c) {
          if (gatew) {
            *(LAS u16*)(size_t)(auxa + (c & 3) * 32768) = (u16)gv[0];
            gv[0] = gv[1]; gv[1] = gv[2]; gv[2] = gv[3]; gv[3] = gv[4]; gv[4] = gv[5]; gv[5] = gv[6];
          }
          BARRIER();
          if (c == TOUCH_AT) {
            const int st = 24;
#pragma unroll 1
            for (int i = idx; i < 147; i += st) touch(i);
          }
        }
        lds_arrive(lds, L_MISC + 12, lane);
      }
    }
    STAMP(2);
    tag = read_tag(lds);
    {
      if (hasB) {
        const unsigned off2 = (unsigned)((lane >> 3) * 896 + (((lane & 7) ^ (((lane >> 4) & 3) << 1)) * 16));
        const char* src2 = (const char*)(P.w2 + 64 * nt) + (unsigned)(p * 112 * 896);
#pragma unroll
        for (int t = 0; t < 14; ++t) dma16(src2 + t * 8 * 896, off2, lds0 + L_R2 + (p * 14 + t) * 1024);
      }
      unsigned off[7];
#pragma unroll
      for (int t = 0; t < 7; ++t) {
        int pp_ = 64 * t + lane;
        int k = (pp_ * 4682) >> 16;
        off[t] = (unsigned)(k * 3584 + 16 * (pp_ - k * 14));
      }
      const char* src3 = (const char*)(P.w3 + ct * 112);
      if (type == 3) {
        const int cg = c1 & 3, strip = rt * 4 + p;
        lds_wait(lds, L_MISC + 32 + 4 * p, 1);
        __builtin_amdgcn_s_setprio(2);
        const unsigned hx = lds0 + p * 6144 + lane * 16;
        const unsigned lo = WS_H1 + ((2 * strip + (r >> 4)) * 14) * 1024 + ((r & 15) + 16 * h) * 16;
#pragma unroll
        for (int f = 0; f < 3; ++f) {
          const f32x4 a0 = *(const LAS f32x4*)(size_t)(hx + 2 * f * 1024), a1 = *(const LAS f32x4*)(size_t)(hx + (2 * f + 1) * 1024);
          u32x4 v = {gelu2(f32x2{a0[0], a0[1]}, 1.f, 1.f), gelu2(f32x2{a0[2], a0[3]}, 1.f, 1.f), gelu2(f32x2{a1[0], a1[1]}, 1.f, 1.f), gelu2(f32x2{a1[2], a1[3]}, 1.f, 1.f)};
          st16(wsr, v, lo, (f < 2) ? (unsigned)((3 * cg + 2) * 1024 + f * 512) : (unsigned)((12 + (cg >> 1)) * 1024 + (cg & 1) * 512));
        }
        lds_arrive(lds, L_MISC + 12, lane);
      }
      WAIT_VM(0);
      if (type == 3) { set_flag(ws, fl_p1(rt, c1, wave), tag, lane); __builtin_amdgcn_s_setprio(0); }
      lds_arrive(lds, L_MISC + 0, lane);
      lds_wait(lds, L_MISC + 12, type == 3 ? 10 : 6);
      STAMP(23);
#pragma unroll 1
      for (int g = p; g < 14; g += 4) {
        int gs = g + (2 * (idx >> 2)) % 14; gs = gs >= 14 ? gs - 14 : gs;
        const char* sg_ = src3 + (unsigned)(gs * 32 * 3584);
#pragma unroll
        for (int t = 0; t < 7; ++t) dma16(sg_, off[t], lds0 + L_W3 + g * 7168 + t * 1024);
      }
    }
    STAMP(4);
  } else {
    __builtin_amdgcn_s_setprio(2);
    const int w4 = wave;
    const int strip = rt * 4 + w4;
    if (wave == 1) {
      const unsigned off = (unsigned)((idx * 16 + (lane & 15)) * 128);
      dma4((const void*)(pc0 & ~127ull), min(off, CODE_BYTES), lds0 + L_SINK);
    }
    if (tid < 128) { *(LAS unsigned*)(lds + L_MISC + tid * 4) = 0u; *(LAS float*)(lds + L_P1S + tid * 4) = 0.f; }
    if (type == 3) {
      *(LAS u16*)(lds + wave * 32768 + 16384 + lane * 256 + ((14 ^ ((lane & 3) << 2)) * 16)) = (u16)0x3F80;
    }
    const int blk = (lane >> 4) & 1;
    const int ustride = (type == 3) ? 64 : 56;
    unsigned xa[4], wa[4];
#pragma unroll
    for (int i = 0; i < 4; ++i) {
      const int s4 = i;
      xa[i] = lds0 + (w4 * 32 + r) * 128 + (((2 * s4 + h) ^ ((r >> 1) & 7)) * 16);
      int bo = ustride * i + 32 * blk + 8 * pp;
      wa[i] = lds0 + 16384 + (8 * h + q) * 256 + (((bo >> 6) ^ q) << 6) + (bo & 63);
    }
    f32x16 acc[4];
#pragma unroll
    for (int u = 0; u < 4; ++u) acc[u] = splat16(0.f);
    if (wave == 0) {
      u64 ep = __hip_atomic_load((GAS u64*)(ws + WS_EPOCH), RLX_AGENT);
      if (lane == 0) *(LAS u64*)(lds + L_TAG) = ep + 1;
    }

    f32x16 gram = splat16(0.f);
    if (type == 2) p1_consume<2>(xa, wa, acc, gram); else if (type == 3) p1_consume<3>(xa, wa, acc, gram); else p1_consume<0>(xa, wa, acc, gram);
    STAMP(2);
    if (type == 2) {
#pragma unroll
      for (int u = 0; u < 4; ++u) {
        int head = (c1 & 3) * 4 + u;
#pragma unroll
        for (int s = 0; s < 2; ++s) {
          u32x4 v = pack8(acc[u], s);
          if (r >= 28) { unsigned cst = (r == 28) ? 0x3F803F80u : 0u; v[0] = cst; v[1] = cst; v[2] = cst; v[3] = cst; }
          st16(wsr, v, lane * 16, WS_V + ((head * 64 + strip) * 2 + s) * 1024);
        }
      }
    } else if (type < 2) {
      if (c1 == 4) {
        float glog = acc[3][12];
        float g = __builtin_amdgcn_rcpf(1.f + __builtin_amdgcn_exp2f(-1.4426950408889634f * glog));
        if (h == 1) st4(wsr, __float_as_uint(g), r * 4, WS_G + strip * 128);
      }
      const float sc = (type == 0) ? (0.1889822365046136f * 1.4426950408889634f) : 1.f;
      const unsigned dst = (type == 0 ? WS_Q : WS_K);
#pragma unroll
      for (int u = 0; u < 4; ++u) {
        int head = (c1 & 3) * 4 + u;
#pragma unroll
        for (int k = 0; k < 16; ++k) acc[u][k] *= sc;
#pragma unroll
        for (int s = 0; s < 2; ++s) {
          u32x4 v = pack8(acc[u], s);
          if (s == 1 && h == 1) { v[2] = 0u; v[3] = 0u; }
          st16(wsr, v, lane * 16, dst + ((head * 64 + strip) * 2 + s) * 1024);
        }
      }
    } else {
      LAS float* csl = (LAS float*)(lds + L_P1S);
      const float sx = swap32_add(h ? 0.f : acc[3][8]);
      float dg = 0.f;
      const int dreg = (r & 3) + 4 * (r >> 3);
#pragma unroll
      for (int k = 0; k < 16; ++k) dg = (dreg == k) ? gram[k] : dg;
      const float sxx = swap32_add((h == ((r >> 2) & 1)) ? dg : 0.f);
      const float mean = sx * (1.f / 448.f);
      const float rstd = __builtin_amdgcn_rsqf(fmaxf(sxx * (1.f / 448.f) - mean * mean, 0.f) + 1e-5f);
      u32x4 bm = {0u, 0u, 0u, 0u};
      {
        __bf16 mh = (__bf16)(-mean);
        float ml = -mean - (float)mh;
        bm[0] = h ? 0u : pk2((float)mh, (float)mh);
        bm[1] = h ? 0u : pk2(ml, 0.f);
      }
      lds_wait(lds, L_MISC + 16, 2);
#pragma unroll
      for (int u = 3; u >= 0; --u) {
        float c = csl[u * 32 + r];
        __bf16 chh = (__bf16)c;
        float cl = c - (float)chh;
        u32x4 am = {pk2((float)chh, cl), pk2((float)chh, 0.f), 0u, 0u};
        acc[u] = MFMA32(as_bf8(am), as_bf8(bm), acc[u]);
      }
      const int tok = strip * 32 + r - shift;
      const float rs = (tok >= 0 && tok < S_) ? rstd : 0.f;
      {
        const unsigned hx = lds0 + wave * 6144 + lane * 16;
#pragma unroll
        for (int k = 0; k < 6; ++k) {
          const f32x16& A = acc[2 + (k >> 2)];
          *(LAS f32x4*)(size_t)(hx + k * 1024) = f32x4{A[4 * (k & 3)] * rs, A[4 * (k & 3) + 1] * rs, A[4 * (k & 3) + 2] * rs, A[4 * (k & 3) + 3] * rs};
        }
        lds_arrive(lds, L_MISC + 32 + 4 * wave, lane);
      }
      const unsigned lo = WS_H1 + ((2 * strip + (r >> 4)) * 14 + 3 * (c1 & 3)) * 1024 + ((r & 15) + 16 * h) * 16;
#pragma unroll
      for (int f = 0; f < 4; ++f) {
        const int u = f >> 1, s = f & 1;
        u32x4 v;
#pragma unroll
        for (int j = 0; j < 4; ++j) v[j] = gelu2(f32x2{acc[u][8 * s + 2 * j], acc[u][8 * s + 2 * j + 1]}, rs, rs);
        st16(wsr, v, lo, u * 1024 + s * 512);
      }
    }
    lds_arrive(lds, L_MISC + 12, lane);
    STAMP(3);
    WAIT_VM(0);
    tag = read_tag(lds);
    set_flag(ws, fl_p1(rt, c1, wave), tag, lane);
    __builtin_amdgcn_s_setprio(0);
    STAMP(4);
  }

  u32x4 wa[28];
  int oflag = -1;
  const int rk4 = (2 * nt + 4 * (idx & 3)) % 14;
  auto k4 = [&](int i) { int k = i + rk4; return k >= 14 ? k - 14 : k; };
  const int rk3 = (2 * (idx >> 2)) % 14;
  if (wave < 4) {
    if (hasB) {
      const int s16 = 4 * mt + wave;
      u32x2 xr[4];
      {
        const u16* xp = P.x + (size_t)(16 * s16 + t16) * D_ + 64 * nt + 4 * q4;
#pragma unroll
        for (int tau = 0; tau < 4; ++tau) xr[tau] = *(const u32x2*)(xp + 16 * tau);
      }
      poll_flags(ws, lane < 8 ? fl_p1(mt >> 1, 12 + (lane & 3), ((s16 >> 1) & 3) + 4 * (lane >> 2)) : -1, tag);
      __builtin_amdgcn_s_setprio(2);
      STAMP(5);
      u32x4 hf[14];
#pragma unroll
      for (int s = 0; s < 14; ++s) hf[s] = ld16(wsr, lane * 16, WS_H1 + (s16 * 14 + s) * 1024);
      lds_wait(lds, L_MISC + 0, 4);
      const int z = 2 * (q4 & 1) + (q >> 1);
      unsigned a0[4], a1[4];
#pragma unroll
      for (int tau = 0; tau < 4; ++tau) {
        a0[tau] = lds0 + L_R2 + (16 * (q4 >> 1) + 4 * (q4 & 1) + q) * 128 + ((tau ^ z) * 32) + 8 * pp;
        a1[tau] = lds0 + L_R2 + (96 + 112 * (q4 >> 1) + 4 * (q4 & 1) + q) * 128 + ((tau ^ z) * 32) + 8 * pp;
      }
      f32x4 acc[4];
      bf16x8 wf[4];
#pragma unroll
      for (int tau = 0; tau < 4; ++tau) { acc[tau] = f32x4{0.f, 0.f, 0.f, 0.f}; wf[tau] = trf(a0[tau], 1024); }
#pragma unroll
      for (int s = 0; s < 14; ++s) {
#pragma unroll
        for (int tau = 0; tau < 4; ++tau) {
          acc[tau] = MFMA16(wf[tau], as_bf8(hf[s]), acc[tau]);
          const int n = s + 1;
          if (n < 12) wf[tau] = trf(a0[tau] + (112 * (n / 3) + 32 * (n % 3)) * 128, 1024);
          else if (n < 14) wf[tau] = trf(a1[tau] + (224 * (n - 12)) * 128, 1024);
          SB0;
        }
      }
      STAMP(6);
      unsigned v[8];
      float sx = 0.f, sxx = 0.f;
#pragma unroll
      for (int tau = 0; tau < 4; ++tau)
#pragma unroll
        for (int k = 0; k < 2; ++k) {
          unsigned w = pk2(acc[tau][2 * k] + bflo(xr[tau][k]), acc[tau][2 * k + 1] + bfhi(xr[tau][k]));
          v[2 * tau + k] = w;
          sx = dot2(w, 0x3F803F80u, sx);
          sxx = dot2(w, w, sxx);
        }
      st16(wsr, u32x4{v[0], v[1], v[2], v[3]}, lane * 16, WS_X1 + (s16 * 14 + 2 * nt) * 1024);
      st16(wsr, u32x4{v[4], v[5], v[6], v[7]}, lane * 16, WS_X1 + (s16 * 14 + 2 * nt + 1) * 1024);
      sx = swap16_add(swap32_add(sx)); sxx = swap16_add(swap32_add(sxx));
      if (q4 == 0) st8(wsr, u32x2{__float_as_uint(sx), __float_as_uint(sxx)}, t16 * 8, WS_ST + (s16 * 8 + nt) * 128);
      WAIT_VM(0);
      set_flag(ws, FL_X1 + (mt * 7 + nt) * 4 + wave, tag, lane);
      __builtin_amdgcn_s_setprio(0);
      STAMP(7);
    }
    lds_wait(lds, L_MISC + 4, 4);
    {
      const unsigned ab = lds0 + L_W3 + (4 * q4 + q) * 224 + 8 * pp + wave * 32;
      const u32x4 ones = {0x3F803F80u, 0x3F803F80u, 0x3F803F80u, 0x3F803F80u};
      f32x4 c0 = {0.f, 0.f, 0.f, 0.f}, c1 = c0;
#pragma unroll
      for (int s = 0; s < 14; ++s) {
        c0 = MFMA16(as_bf8(ones), trf(ab + s * 7168, 3584), c0);
        c1 = MFMA16(as_bf8(ones), trf(ab + s * 7168 + 128, 3584), c1);
      }
      if (q4 < 2) *(LAS float*)(lds + L_CS3 + ((wave + 4 * q4) * 16 + t16) * 4) = q4 ? c1[0] : c0[0];
      lds_arrive(lds, L_MISC + 24, lane);
    }
    STAMP(8);
  } else {
    const int head = 2 * xcd + (idx & 1), qs = (idx >> 1) * 4 + (wave - 4);
    {
      int fi = -1;
      const int hw = 0;
      if (lane < 18) {
        int ks = qs - 8 + (lane < 9 ? lane : lane - 9);
        if (ks >= 0) fi = fl_p1(ks >> 2, (lane < 9 ? 4 : 8) + (head >> 2), (ks & 3) + hw);
      } else if (lane == 18) fi = fl_p1(qs >> 2, head >> 2, (qs & 3) + hw);
      else if (lane == 19) fi = fl_p1(qs >> 2, 4, qs & 3);
      poll_flags(ws, fi, tag);
    }
    lds_arrive(lds, L_MISC + 4, lane);
    STAMP(5);
    const int t0 = max(8 - qs, 0);
    u32x4 qf0 = ld16(wsr, lane * 16, WS_Q + ((head * 64 + qs) * 2) * 1024);
    u32x4 qf1 = ld16(wsr, lane * 16 + 1024, WS_Q + ((head * 64 + qs) * 2) * 1024);
    u32x4 kf[9][2], vf[9][2];
#pragma unroll
    for (int t = 0; t < 9; ++t) {
      const unsigned so = (unsigned)(head * 64 + max(qs - 8 + t, 0)) * 2048u;
      kf[t][0] = ld16(wsr, lane * 16, WS_K + so);
      kf[t][1] = ld16(wsr, lane * 16 + 1024, WS_K + so);
    }
#pragma unroll
    for (int t = 0; t < 9; ++t) {
      const unsigned so = (unsigned)(head * 64 + max(qs - 8 + t, 0)) * 2048u;
      vf[t][0] = ld16(wsr, lane * 16, WS_V + so);
      vf[t][1] = ld16(wsr, lane * 16 + 1024, WS_V + so);
    }
    const float gq = __uint_as_float(ld4(wsr, r * 4, WS_G + qs * 128));
    STAMP(25);
    const float NEG = -1e30f;
    const int rk = r - 4 * h;
    float m = NEG;
#pragma unroll
    for (int t = 0; t < 9; ++t) {
      f32x16 s = MFMA32(as_bf8(kf[t][0]), as_bf8(qf0), splat16(0.f));
      s = MFMA32(as_bf8(kf[t][1]), as_bf8(qf1), s);
      if (t == 0 || t == 8) {
#pragma unroll
        for (int i = 0; i < 16; ++i) {
          const int key = (i & 3) + 8 * (i >> 2);
          s[i] = ((t == 0) ? (key > rk) : (key <= rk)) ? s[i] : NEG;
        }
      }
      float ma = max3f(s[0], s[1], s[2]), mb = max3f(s[3], s[4], s[5]);
      ma = max3f(ma, s[6], s[7]); mb = max3f(mb, s[8], s[9]);
      ma = max3f(ma, s[10], s[11]); mb = max3f(mb, s[12], s[13]);
      ma = max3f(ma, s[14], s[15]);
      float mn = max3f(m, ma, mb);
      m = (t == 8 || t >= t0) ? mn : m;
    }
    m = swap32_max(m);
    STAMP(26);
    const f32x16 negm = splat16(-m);
    f32x16 o = splat16(0.f);
    f32x16 sc = MFMA32(as_bf8(kf[0][0]), as_bf8(qf0), negm);
    sc = MFMA32(as_bf8(kf[0][1]), as_bf8(qf1), sc);
#pragma unroll
    for (int t = 0; t < 9; ++t) {
      f32x16 sn;
      if (t < 8) {
        sn = MFMA32(as_bf8(kf[t + 1][0]), as_bf8(qf0), negm);
        sn = MFMA32(as_bf8(kf[t + 1][1]), as_bf8(qf1), sn);
      }
      SB0;
      if (t == 8 || t >= t0) {
#pragma unroll
        for (int i = 0; i < 16; ++i) {
          const int key = (i & 3) + 8 * (i >> 2);
          float v = __builtin_amdgcn_exp2f(sc[i]);
          if (t == 0) v = (key > rk) ? v : 0.f;
          if (t == 8) v = (key <= rk) ? v : 0.f;
          sc[i] = v;
        }
        o = MFMA32(as_bf8(vf[t][0]), as_bf8(pack8(sc, 0)), o);
        o = MFMA32(as_bf8(vf[t][1]), as_bf8(pack8(sc, 1)), o);
      }
      if (t < 8) sc = sn;
    }
    STAMP(27);
    float l = (h == 1) ? o[12] : 0.f;
    l = swap32_add(l);
    const float f = gq * __builtin_amdgcn_rcpf(l);
#pragma unroll
    for (int i = 0; i < 16; ++i) o[i] *= f;
    u32x4 o0 = pack8(o, 0), o1 = pack8(o, 1);
    if (h == 1) { o1[2] = 0u; o1[3] = 0u; }
    st16(wsr, o0, lane * 16, WS_O + ((qs * 16 + head) * 2) * 1024);
    st16(wsr, o1, lane * 16, WS_O + ((qs * 16 + head) * 2 + 1) * 1024);
    oflag = FL_O + qs * 16 + head;
    STAMP(7);
    lds_wait(lds, L_MISC + 4, 4);
    STAMP(8);
  }

  {
    const int s16 = rt * 8 + wave;
    {
      int fi = -1;
      if (lane < 7) fi = FL_X1 + ((s16 >> 2) * 7 + lane) * 4 + (s16 & 3);
      else if (lane == 7) fi = fl_p1(rt, 4, wave >> 1);
      else if (lane < 10) fi = FL_SV + nt * 16 + 2 * wave + (lane - 8);
      poll_flags_pub(ws, fi, tag, oflag, lane);
    }
    STAMP(9);
    u32x4 xf[14];
#pragma unroll
    for (int s = 0; s < 14; ++s) { int k = s + rk3; k = k >= 14 ? k - 14 : k; xf[s] = ld16(wsr, lane * 16, WS_X1 + (s16 * 14 + k) * 1024); }
    u32x2 st0 = ld8(wsr, q4 * 128 + t16 * 8, WS_ST + s16 * 1024);
    u32x2 st1 = ld8(wsr, min(q4 + 4, 6) * 128 + t16 * 8, WS_ST + s16 * 1024);
    const float g = __uint_as_float(ld4(wsr, t16 * 4, WS_G + s16 * 64));
    const unsigned ab = lds0 + L_W3 + (4 * q4 + q) * 224 + 8 * pp;
    f32x4 acc[7];
    bf16x8 wf[7];
#pragma unroll
    for (int tau = 0; tau < 7; ++tau) { acc[tau] = f32x4{0.f, 0.f, 0.f, 0.f}; wf[tau] = trf(ab + tau * 32, 3584); }
#pragma unroll
    for (int s = 0; s < 14; ++s) {
#pragma unroll
      for (int tau = 0; tau < 7; ++tau) {
        acc[tau] = MFMA16(wf[tau], as_bf8(xf[s]), acc[tau]);
        if (s < 13) wf[tau] = trf(ab + (s + 1) * 7168 + tau * 32, 3584);
        if (tau == 2 || tau == 5) wa[2 * s + (tau == 5)] = ld16(wsr, lane * 16 + (tau == 5) * 1024, WS_W4T + ((nt * 8 + wave) * 28 + 2 * k4(s)) * 1024);
        SB0;
      }
    }
    lds_arrive(lds, L_MISC + 28, lane);
    STAMP(10);
    float sx = __uint_as_float(st0[0]) + (q4 < 3 ? __uint_as_float(st1[0]) : 0.f);
    float sxx = __uint_as_float(st0[1]) + (q4 < 3 ? __uint_as_float(st1[1]) : 0.f);
    sx = swap16_add(swap32_add(sx));
    sxx = swap16_add(swap32_add(sxx));
    const float mean = sx * (1.f / 448.f);
    const float var = fmaxf(sxx * (1.f / 448.f) - mean * mean, 0.f);
    const float rstd = __builtin_amdgcn_rsqf(var + 1e-5f);
    u32x4 bm = {0u, 0u, 0u, 0u};
    {
      __bf16 mh = (__bf16)(-mean);
      float ml = -mean - (float)mh;
      bm[0] = q4 ? 0u : pk2((float)mh, (float)mh);
      bm[1] = q4 ? 0u : pk2(ml, 0.f);
    }
    lds_wait(lds, L_MISC + 24, 4);
#pragma unroll
    for (int tau = 0; tau < 7; ++tau) {
      float c = *(const LAS float*)(lds + L_CS3 + (tau * 16 + t16) * 4);
      __bf16 chh = (__bf16)c;
      float cl = c - (float)chh;
      u32x4 am = {0u, 0u, 0u, 0u};
      am[0] = pk2((float)chh, cl);
      am[1] = pk2((float)chh, 0.f);
      acc[tau] = MFMA16(as_bf8(am), as_bf8(bm), acc[tau]);
    }
    const float rg = rstd * (1.f - g);
    unsigned yy[7][2];
#pragma unroll
    for (int tau = 0; tau < 7; ++tau) {
      yy[tau][0] = gelu2(f32x2{acc[tau][0], acc[tau][1]}, rstd, rg);
      yy[tau][1] = gelu2(f32x2{acc[tau][2], acc[tau][3]}, rstd, rg);
    }
    const unsigned dst = WS_H2 + (((s16 >> 1) * 16 + ct) * 7) * 1024;
    const int tr = 16 * (s16 & 1) + t16;
#pragma unroll
    for (int i = 0; i < 3; ++i) {
      u32x4 v = {yy[2 * i][0], yy[2 * i][1], yy[2 * i + 1][0], yy[2 * i + 1][1]};
      st16(wsr, v, (q4 >> 1) * 1024 + ((q4 & 1) * 32 + tr) * 16, dst + 2 * i * 1024);
    }
    {
      auto r0 = __builtin_amdgcn_permlane16_swap(yy[6][0], yy[6][0], false, false);
      auto r1 = __builtin_amdgcn_permlane16_swap(yy[6][1], yy[6][1], false, false);
      u32x4 v = {yy[6][0], yy[6][1], r0[1], r1[1]};
      if (!(q4 & 1)) st16(wsr, v, ((q4 >> 1) * 32 + tr) * 16, dst + 6 * 1024);
    }
    STAMP(11);
    WAIT_VM(0);
    set_flag(ws, FL_H2 + (rt * 16 + ct) * 8 + wave, tag, lane);
    STAMP(12);
  }

  if (hasB) {
    f32x16 acc[2][2];
#pragma unroll
    for (int i = 0; i < 2; ++i)
#pragma unroll
      for (int t = 0; t < 2; ++t) acc[i][t] = splat16(0.f);
    const int ts = wave >> 2, a_own = wave & 3;
    u32x2 x1v[2];
    float og;
    {
      poll_flags(ws, lane < 4 ? (int)(FL_O + (2 * mt + (lane & 1)) * 16 + 2 * wave + (lane >> 1)) : (lane == 4 ? (int)(FL_SV + 112 + nt * 4 + (wave >> 1)) : -1), tag);
      u32x4 of[2][4], wo_[8];
#pragma unroll
      for (int ks = 0; ks < 4; ++ks) {
#pragma unroll
        for (int i = 0; i < 2; ++i) wo_[2 * ks + i] = ld16(wsr, lane * 16, WS_WOT + ((nt * 8 + wave) * 8 + 2 * ks + i) * 1024);
#pragma unroll
        for (int t = 0; t < 2; ++t) of[t][ks] = ld16(wsr, lane * 16, WS_O + (((2 * mt + t) * 16 + 2 * wave) * 2 + ks) * 1024);
      }
#pragma unroll
      for (int i = 0; i < 2; ++i)
        x1v[i] = ld8(wsr, ((r & 15) + 16 * (2 * (a_own & 1) + h)) * 16 + (a_own >> 1) * 8 + (r >> 4) * 14 * 1024,
                     WS_X1 + ((4 * mt + 2 * ts) * 14 + 2 * nt + i) * 1024);
      og = 1.f - __uint_as_float(ld4(wsr, r * 4, WS_G + (2 * mt + ts) * 128));
#pragma unroll
      for (int ks = 0; ks < 4; ++ks)
#pragma unroll
        for (int t = 0; t < 2; ++t) {
          acc[0][t] = MFMA32(as_bf8(wo_[2 * ks]), as_bf8(of[t][ks]), acc[0][t]);
          acc[1][t] = MFMA32(as_bf8(wo_[2 * ks + 1]), as_bf8(of[t][ks]), acc[1][t]);
        }
    }
    STAMP(14);
    poll_flags(ws, lane < 8 ? (int)(FL_H2 + ((mt >> 1) * 16 + 2 * wave + (lane & 1)) * 8 + 4 * (mt & 1) + (lane >> 1)) : -1, tag);
    STAMP(15);
#define H2LD(i) ld16(wsr, lane * 16, h2base + (i / 14) * 112 * 1024 + k4(i % 14) * 1024)
    constexpr int WIN = 11;
    u32x4 hf[28];
    const unsigned h2base = WS_H2 + (((2 * mt) * 16 + 2 * wave) * 7) * 1024;
#pragma unroll
    for (int i = 0; i < WIN; ++i) hf[i] = H2LD(i);
#pragma unroll
    for (int t = 0; t < 2; ++t) {
#pragma unroll
      for (int ks = 0; ks < 14; ++ks) {
        acc[0][t] = MFMA32(as_bf8(wa[2 * ks]), as_bf8(hf[14 * t + ks]), acc[0][t]);
        acc[1][t] = MFMA32(as_bf8(wa[2 * ks + 1]), as_bf8(hf[14 * t + ks]), acc[1][t]);
        const int i = 14 * t + ks + WIN;
        if (i < 28) {
          hf[i] = H2LD(i);
          SB0;
        }
      }
      if (t == 0) lds_wait(lds, L_MISC + 28, 8);
#pragma unroll
      for (int a = 0; a < 4; ++a)
#pragma unroll
        for (int i = 0; i < 2; ++i) {
          const f32x16& A = acc[i][t];
          const int p = 8 * t + 2 * a + i;
          const unsigned ad = lds0 + wave * 16384 + p * 1024;
          *(LAS f32x4*)(size_t)(ad + lane * 16) = f32x4{A[4 * a], A[4 * a + 1], A[4 * a + 2], A[4 * a + 3]};
        }
    }
    STAMP(16);
    BARRIER();
    f32x4 fin[2];
    {
      const unsigned rb = lds0 + 2 * wave * 1024 + lane * 16;
      const unsigned rs = 16384u;
#pragma unroll
      for (int i = 0; i < 2; ++i) {
        f32x4 v[8];
#pragma unroll
        for (int si = 0; si < 8; ++si) v[si] = *(const LAS f32x4*)(size_t)(rb + si * rs + i * 1024);
        fin[i] = ((v[0] + v[1]) + (v[2] + v[3])) + ((v[4] + v[5]) + (v[6] + v[7]));
      }
    }
    unsigned d[2][2];
#pragma unroll
    for (int i = 0; i < 2; ++i)
#pragma unroll
      for (int k = 0; k < 2; ++k)
        d[i][k] = pk2(__builtin_fmaf(og, bflo(x1v[i][k]), fin[i][2 * k]), __builtin_fmaf(og, bfhi(x1v[i][k]), fin[i][2 * k + 1]));
    auto s0 = __builtin_amdgcn_permlane32_swap(d[0][0], d[1][0], false, false);
    auto s1 = __builtin_amdgcn_permlane32_swap(d[0][1], d[1][1], false, false);
    u32x4 o = {s0[0], s1[0], s0[1], s1[1]};
    *(u32x4*)(P.out + (size_t)(64 * mt + 32 * ts + r) * D_ + 64 * nt + 32 * h + 8 * a_own) = o;
    STAMP(17);
  } else if (b == 0 && wave == 0) {
#pragma unroll 1
    for (int i = 0; i < 4; ++i) poll_flags(ws, FL_P1 + (i * 64 + lane) * 8, tag);
    if (lane == 0) __hip_atomic_store((GAS u64*)(ws + WS_EPOCH), tag, RLX_AGENT);
  }
}

extern "C" void kernel_launch(void* const* d_in, const int* in_sizes, int n_in, void* d_out, int out_size, void* d_ws,
                              size_t ws_size, hipStream_t stream) {
  Params P;
  P.x = (const u16*)d_in[0];
  P.wqkv = (const u16*)d_in[1];
  P.wo = (const u16*)d_in[2];
  P.w1 = (const u16*)d_in[4];
  P.w2 = (const u16*)d_in[5];
  P.w3 = (const u16*)d_in[6];
  P.w4 = (const u16*)d_in[7];
  P.gw = (const u16*)d_in[8];
  P.ws = (char*)d_ws;
  P.out = (u16*)d_out;
  (void)hipFuncSetAttribute((const void*)mega_kernel, hipFuncAttributeMaxDynamicSharedMemorySize, LDS_BYTES);
  mega_kernel<<<256, 512, LDS_BYTES, stream>>>(P);
}
```

```cpp
#include <hip/hip_runtime.h>
#include <hip/hip_bf16.h>
#include <stdint.h>

typedef __attribute__((ext_vector_type(8))) __bf16 bf16x8;
typedef __attribute__((ext_vector_type(4))) __bf16 bf16x4;
typedef __attribute__((ext_vector_type(2))) __bf16 bf16x2;
typedef __attribute__((ext_vector_type(16))) float f32x16;
typedef __attribute__((ext_vector_type(4))) float f32x4;
typedef __attribute__((ext_vector_type(2))) float f32x2;
typedef __attribute__((ext_vector_type(4))) unsigned u32x4;
typedef __attribute__((ext_vector_type(2))) unsigned u32x2;
typedef unsigned short u16;
typedef unsigned long long u64;
#define LAS __attribute__((address_space(3)))
#define GAS __attribute__((address_space(1)))
#define DI __device__ __forceinline__
#define RLX_AGENT __ATOMIC_RELAXED, __HIP_MEMORY_SCOPE_AGENT

constexpr int S_ = 2048, D_ = 448, F_ = 1792;

constexpr unsigned WS_EPOCH = 0;
constexpr unsigned WS_FL = 256;
constexpr unsigned FL_P1 = 0, FL_X1 = 2048, FL_O = 3072, FL_H2 = 4096, FL_SV = 6144, FL_N = 6400;
constexpr unsigned WS_Q = 1u << 20;
constexpr unsigned WS_K = WS_Q + 16 * 64 * 2048;
constexpr unsigned WS_V = WS_K + 16 * 64 * 2048;
constexpr unsigned WS_H1 = WS_V + 16 * 64 * 2048;
constexpr unsigned WS_X1 = WS_H1 + 128 * 14 * 1024;
constexpr unsigned WS_ST = WS_X1 + 128 * 14 * 1024;
constexpr unsigned WS_O = WS_ST + 128 * 8 * 128;
constexpr unsigned WS_H2 = WS_O + 64 * 32 * 1024;
constexpr unsigned WS_G = WS_H2 + 64 * 112 * 1024;
constexpr unsigned WS_W4T = WS_G + 2048 * 4;
constexpr unsigned WS_WOT = WS_W4T + 7 * 8 * 28 * 1024;
constexpr unsigned WS_CS = WS_WOT + 7 * 8 * 8 * 1024;
constexpr unsigned WS_END = WS_CS + 1792 * 4;

constexpr int L_W3 = 0;
constexpr int L_R2 = 100608;
constexpr int L_MISC = L_R2 + 57344;
constexpr int L_P1S = L_MISC + 512;
constexpr int L_CS3 = L_P1S + 2048;
constexpr int L_SINK = L_CS3 + 512;
constexpr int L_TAG = L_SINK + 256;
constexpr int LDS_BYTES = 160 * 1024;
constexpr unsigned CODE_BYTES = 36 * 1024 + 512;

#define STAMP(i)

#define MFMA32(a, b, c) __builtin_amdgcn_mfma_f32_32x32x16_bf16((a), (b), (c), 0, 0, 0)
#define MFMA16(a, b, c) __builtin_amdgcn_mfma_f32_16x16x32_bf16((a), (b), (c), 0, 0, 0)
#define WAIT_VM(n) asm volatile("s_waitcnt vmcnt(" #n ")" ::: "memory")
#define WAIT_LGKM0 asm volatile("s_waitcnt lgkmcnt(0)" ::: "memory")
#define BARRIER() do { asm volatile("s_waitcnt lgkmcnt(0)" ::: "memory"); __builtin_amdgcn_s_barrier(); asm volatile("" ::: "memory"); } while (0)

DI bf16x8 as_bf8(u32x4 v) { return __builtin_bit_cast(bf16x8, v); }
DI unsigned pk2(float lo, float hi) {
  bf16x2 r = {(__bf16)lo, (__bf16)hi};
  return __builtin_bit_cast(unsigned, r);
}
DI float bflo(unsigned u) { return __uint_as_float(u << 16); }
DI float bfhi(unsigned u) { return __uint_as_float(u & 0xffff0000u); }
DI float dot2(unsigned a, unsigned b, float c) {
  return __builtin_amdgcn_fdot2_f32_bf16(__builtin_bit_cast(bf16x2, a), __builtin_bit_cast(bf16x2, b), c, false);
}
DI float swap32_add(float v) {
  unsigned u = __float_as_uint(v);
  auto r = __builtin_amdgcn_permlane32_swap(u, u, false, false);
  return __uint_as_float(r[0]) + __uint_as_float(r[1]);
}
DI float swap16_add(float v) {
  unsigned u = __float_as_uint(v);
  auto r = __builtin_amdgcn_permlane16_swap(u, u, false, false);
  return __uint_as_float(r[0]) + __uint_as_float(r[1]);
}
DI float swap32_max(float v) {
  unsigned u = __float_as_uint(v);
  auto r = __builtin_amdgcn_permlane32_swap(u, u, false, false);
  return fmaxf(__uint_as_float(r[0]), __uint_as_float(r[1]));
}
DI float gelu_sig(float x) {
  const float c0 = -2.f * 0.7978845608028654f * 1.4426950408889634f;
  const float c1 = c0 * 0.044715f;
  float t = x * x;
  float p = __builtin_fmaf(t, c1, c0);
  float e = __builtin_amdgcn_exp2f(x * p);
  return __builtin_amdgcn_rcpf(1.f + e);
}
DI unsigned gelu2(f32x2 a, float si, float so) {
  const float c0 = -2.f * 0.7978845608028654f * 1.4426950408889634f;
  const float c1 = c0 * 0.044715f;
  f32x2 x = a * si;
  f32x2 t = x * x;
  f32x2 p = __builtin_elementwise_fma(t, f32x2{c1, c1}, f32x2{c0, c0});
  f32x2 z = x * p;
  f32x2 e = {__builtin_amdgcn_exp2f(z[0]), __builtin_amdgcn_exp2f(z[1])};
  f32x2 d = e + 1.f;
  f32x2 rr = {__builtin_amdgcn_rcpf(d[0]), __builtin_amdgcn_rcpf(d[1])};
  f32x2 y = (a * so) * rr;
  return pk2(y[0], y[1]);
}
DI u32x4 pack8(const f32x16& a, int s) {
  u32x4 r;
  r[0] = pk2(a[8 * s + 0], a[8 * s + 1]);
  r[1] = pk2(a[8 * s + 2], a[8 * s + 3]);
  r[2] = pk2(a[8 * s + 4], a[8 * s + 5]);
  r[3] = pk2(a[8 * s + 6], a[8 * s + 7]);
  return r;
}
DI f32x16 splat16(float v) {
  f32x16 z;
#pragma unroll
  for (int i = 0; i < 16; ++i) z[i] = v;
  return z;
}
DI void dma16(const void* sbase, unsigned voff, unsigned ldst) {
  asm volatile("s_mov_b32 m0, %2\n\ts_nop 0\n\tglobal_load_lds_dwordx4 %0, %1" :: "v"(voff), "s"(sbase), "s"(ldst) : "memory");
}
DI void dma4(const void* sbase, unsigned voff, unsigned ldst) {
  asm volatile("s_mov_b32 m0, %2\n\ts_nop 0\n\tglobal_load_lds_dword %0, %1" :: "v"(voff), "s"(sbase), "s"(ldst) : "memory");
}
DI bf16x8 tr_frag(const LAS char* p, int off2) {
  bf16x4 a = __builtin_amdgcn_ds_read_tr16_b64_v4bf16((LAS bf16x4*)(p));
  bf16x4 b = __builtin_amdgcn_ds_read_tr16_b64_v4bf16((LAS bf16x4*)(p + off2));
  return __builtin_shufflevector(a, b, 0, 1, 2, 3, 4, 5, 6, 7);
}
typedef __amdgpu_buffer_rsrc_t rsrc_t;
DI u32x4 ld16(rsrc_t rs, unsigned voff, unsigned soff) { return __builtin_amdgcn_raw_buffer_load_b128(rs, voff, soff, 16); }
DI u32x2 ld8(rsrc_t rs, unsigned voff, unsigned soff) { return __builtin_amdgcn_raw_buffer_load_b64(rs, voff, soff, 16); }
DI unsigned ld4(rsrc_t rs, unsigned voff, unsigned soff) { return __builtin_amdgcn_raw_buffer_load_b32(rs, voff, soff, 16); }
DI void st16(rsrc_t rs, u32x4 v, unsigned voff, unsigned soff) { __builtin_amdgcn_raw_buffer_store_b128(v, rs, voff + soff, 0, 16);    }
DI void st8(rsrc_t rs, u32x2 v, unsigned voff, unsigned soff) { __builtin_amdgcn_raw_buffer_store_b64(v, rs, voff, soff, 16); }
DI void st4(rsrc_t rs, unsigned v, unsigned voff, unsigned soff) { __builtin_amdgcn_raw_buffer_store_b32(v, rs, voff, soff, 16); }

DI void set_flag(char* ws, unsigned idx, u64 tag, int lane) {
  if (lane == 0) __hip_atomic_store((GAS u64*)(ws + WS_FL + (size_t)idx * 128), tag, RLX_AGENT);
}
DI void poll_flags(char* ws, int idx, u64 tag) {
  GAS u64* p = (GAS u64*)(ws + WS_FL + (size_t)max(idx, 0) * 128);
#pragma unroll 1
  for (int spins = 0; spins < (1 << 16); ++spins) {
    u64 v = __hip_atomic_load(p, RLX_AGENT);
    if (__all(idx < 0 || v == tag)) break;
    __builtin_amdgcn_s_sleep(4);
  }
  asm volatile("" ::: "memory");
}
DI void lds_arrive(LAS char* lds, int off, int lane) {
  WAIT_LGKM0;
  if (lane == 0) __hip_atomic_fetch_add((LAS unsigned*)(lds + off), 1u, __ATOMIC_RELAXED, __HIP_MEMORY_SCOPE_WORKGROUP);
}
DI void lds_wait(LAS char* lds, int off, unsigned n) {
#pragma unroll 1
  for (int spins = 0; spins < (1 << 20); ++spins) {
    unsigned v = __hip_atomic_load((LAS unsigned*)(lds + off), __ATOMIC_RELAXED, __HIP_MEMORY_SCOPE_WORKGROUP);
    if (__builtin_amdgcn_readfirstlane(v) >= n) break;
    __builtin_amdgcn_s_sleep(1);
  }
  asm volatile("" ::: "memory");
}
DI u64 read_tag(LAS char* lds) {
  u32x2 t = *(volatile LAS u32x2*)(lds + L_TAG);
  return (u64)__builtin_amdgcn_readfirstlane(t[0]) | ((u64)__builtin_amdgcn_readfirstlane(t[1]) << 32);
}
DI int fl_p1(int rt, int ct, int wv) { return FL_P1 + (rt * 16 + ct) * 8 + wv; }

struct Params {
  const u16 *x, *wqkv, *wo, *w1, *w2, *w3, *w4, *gw;
  char* ws;
  u16* out;
};
#define SB0 __builtin_amdgcn_sched_barrier(0)

DI u32x4 lds16(unsigned a) { return *(const LAS u32x4*)(size_t)a; }
DI bf16x8 trf(unsigned a, int off2) { return tr_frag((const LAS char*)(size_t)a, off2); }

#define TICK(v)
template <int TYPE>
DI void p1_consume(const unsigned (&xa)[4], const unsigned (&wa)[4], f32x16 (&acc)[4]) {
  u32x4 xf[2];
  bf16x8 wf[2][4];
  BARRIER();
#pragma unroll
  for (int i = 0; i < 2; ++i) {
    xf[i] = lds16(xa[i]);
#pragma unroll
    for (int u = 0; u < 4; ++u) wf[i][u] = trf(wa[u] + i * 4096, 1024);
  }
  unsigned wb[4], xb[4];
#pragma unroll
  for (int u = 0; u < 4; ++u) { wb[u] = wa[u]; xb[u] = xa[u]; }
#pragma unroll 1
  for (int c = 0; c < 7; ++c) {
#pragma unroll
    for (int i = 0; i < 4; ++i) {
      if (i == 2) {
        const unsigned sn = ((c + 1) & 3) * 32768;
#pragma unroll
        for (int u = 0; u < 4; ++u) { wb[u] = wa[u] + sn; xb[u] = xa[u] + sn; }
        asm volatile("" : "+v"(wb[0]), "+v"(wb[1]), "+v"(wb[2]), "+v"(wb[3]), "+v"(xb[0]), "+v"(xb[1]), "+v"(xb[2]), "+v"(xb[3]));
        if (c < 6) BARRIER();
      }
      const int ni = (i + 2) & 3;
      const u32x4 xc = xf[i & 1];
      SB0;
#pragma unroll
      for (int u = 0; u < 4; ++u) {
        if (TYPE == 2) acc[u] = MFMA32(as_bf8(xc), wf[i & 1][u], acc[u]);
        else acc[u] = MFMA32(wf[i & 1][u], as_bf8(xc), acc[u]);
        SB0;
        wf[i & 1][u] = trf(wb[u] + ni * 4096, 1024);
        SB0;
      }
      xf[i & 1] = lds16(xb[ni]);
      SB0;
    }
  }
}

DI void w4_issue(const char* src, unsigned soff, unsigned dst) {
#pragma unroll
  for (int j = 0; j < 14; ++j) dma16(src + j * 8 * 896, soff, dst + j * 1024);
}
DI void w4_read(bf16x8 (&wa)[14][2], int k0, unsigned stg, const unsigned (&lb)[2], int h) {
#pragma unroll
  for (int kp = 0; kp < 7; ++kp)
#pragma unroll
    for (int i = 0; i < 2; ++i) {
      if (kp < 6) wa[k0 + kp][i] = trf(stg + lb[i] + (32 * (kp >> 1) + 8 * (kp & 1)) * 128, 16 * 128);
      else wa[k0 + kp][i] = trf(stg + lb[i] + 4 * h * 128 + 96 * 128, 4 * 128);
    }
}

__global__ __launch_bounds__(512, 2) void mega_kernel(Params P) {
  u64 pc0;
  asm volatile("s_getpc_b64 %0" : "=s"(pc0));
  extern __shared__ __attribute__((aligned(16))) char lds_raw[];
  LAS char* lds = (LAS char*)lds_raw;
  const unsigned lds0 = (unsigned)(size_t)lds;
  char* ws = P.ws;
  const int tid = threadIdx.x, lane = tid & 63, wave = __builtin_amdgcn_readfirstlane(tid >> 6);
  const int r = lane & 31, h = lane >> 5;
  const int b = blockIdx.x, xcd = b & 7, idx = b >> 3;
  const int rt = (xcd >> 1) * 4 + (idx & 3);
  const int ct = (xcd & 1) * 8 + (idx >> 2);
  const bool hasB = idx >= 4;
  const int mt = xcd * 4 + (idx & 3);
  const int nt = max((idx >> 2) - 1, 0);
  const rsrc_t wsr = __builtin_amdgcn_make_buffer_rsrc(ws, 0, WS_END, 0x00020000);
  STAMP(0);

  const int t16 = lane & 15, q4 = lane >> 4, q = (lane & 15) >> 2, pp = lane & 3;
  unsigned lb[2];
#pragma unroll
  for (int i = 0; i < 2; ++i) lb[i] = q * 128 + ((i ^ (q >> 1)) * 64) + 32 * ((lane >> 4) & 1) + 8 * pp + 4 * h * 128;
  const unsigned soff = (unsigned)((lane >> 3) * 896 + (((lane & 7) ^ (((lane >> 4) & 1) << 2)) * 16));
  u64 tag;
  const int type = ct >> 2;
  int shift = 0;
  if (type == 3) { int g = ct & 3; shift = (g & 2) ? 2 : 1; shift = (g & 1) ? -shift : shift; }
  if (__builtin_expect(wave >= 4, 1)) {
    const int p = wave - 4;
    const int rot = (2 * (rt & 3) + ct) % 7;
    if (__builtin_expect(p < 2, 1)) {
      const char* xsrc = (const char*)P.x;
      const char* wsrc = (const char*)((type == 3) ? (P.w1 + (ct & 3) * 112) : (P.wqkv + ct * 112));
      const unsigned ldb = (type == 3) ? 896u : 2688u;
      unsigned xoff[8], woff[8];
      const int sg = (lane & 15) ^ ((lane >> 4) << 2);
#pragma unroll
      for (int i = 0; i < 8; ++i) {
        int row = 64 * p + 8 * i + (lane >> 3);
        int tok = min(max(rt * 128 + row - shift, 0), S_ - 1);
        xoff[i] = (unsigned)tok * 896u + (unsigned)(((lane & 7) ^ ((row >> 1) & 7)) * 16);
        woff[i] = (unsigned)(32 * p + 4 * i + (lane >> 4)) * ldb + (unsigned)sg * 16u;
      }
      const bool wact = sg < 14;
      auto issue = [&](int c) {
        int cc = c + rot; cc = cc >= 7 ? cc - 7 : cc;
        const char* xb = xsrc + cc * 128;
        const char* wb = wsrc + (unsigned)cc * 64u * ldb;
        const unsigned sl = lds0 + (c & 3) * 32768 + p * 8192;
#pragma unroll
        for (int i = 0; i < 8; ++i) dma16(xb, xoff[i], sl + i * 1024);
        if (wact) {
#pragma unroll
          for (int i = 0; i < 8; ++i) dma16(wb, woff[i], sl + 16384 + i * 1024);
        }
      };
      STAMP(20);
      issue(0);
      STAMP(21);
#pragma unroll 1
      for (int c = 0; c < 6; ++c) {
        issue(c + 1);
        WAIT_VM(16);
        __builtin_amdgcn_s_barrier();
        if (c == 0) STAMP(1);
      }
      STAMP(22);
      WAIT_VM(0);
      __builtin_amdgcn_s_barrier();
    } else {
      const int sp = p - 2;
      if (type == 3) {
        float sq[2] = {0.f, 0.f}, cs[4] = {0.f, 0.f, 0.f, 0.f};
        unsigned sxa[4], swa[4];
        const int blk = (lane >> 4) & 1;
#pragma unroll
        for (int i = 0; i < 4; ++i) {
          sxa[i] = lds0 + (sp * 64 + r) * 128 + (((2 * i + h) ^ ((r >> 1) & 7)) * 16);
          swa[i] = lds0 + 16384 + sp * 8192 + (8 * h + q) * 256 + ((i ^ q) << 6) + 32 * blk + 8 * pp;
        }
        WAIT_VM(0);
#pragma unroll 1
        for (int c = 0; c < 7; ++c) {
          BARRIER();
          const unsigned so = (c & 3) * 32768;
#pragma unroll
          for (int t = 0; t < 2; ++t)
#pragma unroll
            for (int i = 0; i < 4; ++i) {
              u32x4 xv = lds16(sxa[i] + so + t * 4096);
              u32x4 wv = __builtin_bit_cast(u32x4, trf(swa[i] + so + t * 4096, 1024));
#pragma unroll
              for (int d = 0; d < 4; ++d) { sq[t] = dot2(xv[d], xv[d], sq[t]); cs[i] = dot2(wv[d], 0x3F803F80u, cs[i]); }
            }
        }
        LAS float* csl = (LAS float*)(lds + L_P1S);
        LAS float* sts = (LAS float*)(lds + L_P1S + 512);
#pragma unroll
        for (int u = 0; u < 4; ++u) cs[u] = swap32_add(cs[u]);
        __hip_atomic_fetch_add(csl + (2 * h) * 32 + r, h ? cs[2] : cs[0], __ATOMIC_RELAXED, __HIP_MEMORY_SCOPE_WORKGROUP);
        __hip_atomic_fetch_add(csl + (2 * h + 1) * 32 + r, h ? cs[3] : cs[1], __ATOMIC_RELAXED, __HIP_MEMORY_SCOPE_WORKGROUP);
        sq[0] = swap32_add(sq[0]); sq[1] = swap32_add(sq[1]);
        sts[sp * 64 + lane] = h ? sq[1] : sq[0];
        lds_arrive(lds, L_MISC + 16, lane);
        lds_arrive(lds, L_MISC + 12, lane);
      } else if (sp == 0) {
        const int job = rt * 12 + ct;
        const bool act = job < 140, is4 = job < 112;
        const int jn = is4 ? (job >> 4) : ((job - 112) >> 2), ju = is4 ? (job & 15) : ((job - 112) & 3);
        const unsigned stg = lds0 + 131072;
        BARRIER();
        if (act) w4_issue((const char*)((is4 ? P.w4 : P.wo) + 64 * jn) + (unsigned)(ju * 112 * 896), soff, stg);
        BARRIER();
        BARRIER();
        BARRIER();
        WAIT_VM(0);
        if (act) {
          if (is4) {
            bf16x8 fr[14][2];
            w4_read(fr, 0, stg, lb, h);
#pragma unroll
            for (int k = 0; k < 14; ++k)
              st16(wsr, __builtin_bit_cast(u32x4, fr[k >> 1][k & 1]), lane * 16, WS_W4T + ((jn * 16 + ju) * 14 + k) * 1024);
          } else {
#pragma unroll
            for (int k = 0; k < 16; ++k) {
              const int s_ = (k >> 1) & 1;
              const unsigned ro = stg + lb[k & 1] + (28 * (k >> 2) + 16 * s_) * 128;
              const int e1 = s_ ? (8 * 128 - 4 * h * 128) : 8 * 128;
              bf16x4 a0 = __builtin_amdgcn_ds_read_tr16_b64_v4bf16((LAS bf16x4*)(size_t)(ro));
              bf16x4 a1 = __builtin_amdgcn_ds_read_tr16_b64_v4bf16((LAS bf16x4*)(size_t)(ro + e1));
              st16(wsr, __builtin_bit_cast(u32x4, __builtin_shufflevector(a0, a1, 0, 1, 2, 3, 4, 5, 6, 7)), lane * 16,
                   WS_WOT + ((jn * 4 + ju) * 16 + k) * 1024);
            }
          }
        }
        BARRIER();
        BARRIER();
        BARRIER();
        WAIT_VM(0);
        if (act) set_flag(ws, FL_SV + job, read_tag(lds), lane);
        lds_arrive(lds, L_MISC + 12, lane);
      } else {
        const bool gatew = (ct == 4);
        unsigned gv[7];
#pragma unroll
        for (int c = 0; c < 7; ++c) {
          int cc = c + rot; cc = cc >= 7 ? cc - 7 : cc;
          gv[c] = P.gw[cc * 64 + lane];
        }
        const unsigned auxa = lds0 + 16384 + lane * 256 + ((14 ^ ((lane & 3) << 2)) * 16);
        asm volatile("" : "+v"(gv[0]), "+v"(gv[1]), "+v"(gv[2]), "+v"(gv[3]), "+v"(gv[4]), "+v"(gv[5]), "+v"(gv[6]));
        WAIT_VM(0);
        auto touch = [&](int i) {
          if (i < 147) {
            const char* base; unsigned off;
            if (i < 49) { base = (const char*)P.w2; off = (unsigned)(i * 64 + lane) * 128u; }
            else { int l = (i - 49) * 64 + lane; int row = (l * 9363) >> 17; base = (const char*)P.w3 + (xcd & 1) * 1792; off = (unsigned)(row * 3584 + (l - row * 14) * 128); }
            dma4(base, off, lds0 + L_SINK);
          }
        };
#pragma unroll 1
        for (int c = 0; c < 7; ++c) {
          if (gatew) {
            *(LAS u16*)(size_t)(auxa + (c & 3) * 32768) = (u16)gv[0];
            gv[0] = gv[1]; gv[1] = gv[2]; gv[2] = gv[3]; gv[3] = gv[4]; gv[4] = gv[5]; gv[5] = gv[6];
          }
          BARRIER();
          if (c > 0) {
            touch((c - 1) * 32 + idx);
            if (xcd & 1) touch((c - 1) * 32 + 16 + idx);
          }
        }
        lds_arrive(lds, L_MISC + 12, lane);
      }
    }
    STAMP(2);
    tag = read_tag(lds);
    {
      if (hasB) {
        const unsigned off2 = (unsigned)((lane >> 3) * 896 + (((lane & 7) ^ (((lane >> 4) & 3) << 1)) * 16));
        const char* src2 = (const char*)(P.w2 + 64 * nt) + (unsigned)(p * 112 * 896);
#pragma unroll
        for (int t = 0; t < 14; ++t) dma16(src2 + t * 8 * 896, off2, lds0 + L_R2 + (p * 14 + t) * 1024);
      }
      unsigned off[7];
#pragma unroll
      for (int t = 0; t < 7; ++t) {
        int pp_ = 64 * t + lane;
        int k = (pp_ * 4682) >> 16;
        off[t] = (unsigned)(k * 3584 + 16 * (pp_ - k * 14));
      }
      const char* src3 = (const char*)(P.w3 + ct * 112);
      WAIT_VM(0);
      lds_arrive(lds, L_MISC + 0, lane);
      lds_wait(lds, L_MISC + 12, 6);
      STAMP(23);
#pragma unroll 1
      for (int g = p; g < 14; g += 4) {
        const char* sg_ = src3 + (unsigned)(g * 32 * 3584);
#pragma unroll
        for (int t = 0; t < 7; ++t) dma16(sg_, off[t], lds0 + L_W3 + g * 7168 + t * 1024);
      }
    }
    STAMP(4);
  } else {
    __builtin_amdgcn_s_setprio(2);
    const int w4 = wave;
    const int strip = rt * 4 + w4;
    if (wave == 1) {
      const unsigned off = (unsigned)((idx * 16 + (lane & 15)) * 128);
      dma4((const void*)(pc0 & ~127ull), min(off, CODE_BYTES), lds0 + L_SINK);
    }
    if (tid < 128) { *(LAS unsigned*)(lds + L_MISC + tid * 4) = 0u; *(LAS float*)(lds + L_P1S + tid * 4) = 0.f; }
    if (type == 3) {
      *(LAS u16*)(lds + wave * 32768 + 16384 + lane * 256 + ((14 ^ ((lane & 3) << 2)) * 16)) = (u16)0x3F80;
    }
    const int blk = (lane >> 4) & 1;
    const int ustride = (type == 3) ? 64 : 56;
    unsigned xa[4], wa[4];
#pragma unroll
    for (int i = 0; i < 4; ++i) {
      const int s4 = i;
      xa[i] = lds0 + (w4 * 32 + r) * 128 + (((2 * s4 + h) ^ ((r >> 1) & 7)) * 16);
      int bo = ustride * i + 32 * blk + 8 * pp;
      wa[i] = lds0 + 16384 + (8 * h + q) * 256 + (((bo >> 6) ^ q) << 6) + (bo & 63);
    }
    f32x16 acc[4];
#pragma unroll
    for (int u = 0; u < 4; ++u) acc[u] = splat16(0.f);
    if (wave == 0) {
      u64 ep = __hip_atomic_load((GAS u64*)(ws + WS_EPOCH), RLX_AGENT);
      if (lane == 0) *(LAS u64*)(lds + L_TAG) = ep + 1;
    }

    if (type == 2) p1_consume<2>(xa, wa, acc); else p1_consume<0>(xa, wa, acc);
    if (type == 3) lds_arrive(lds, L_MISC + 12, lane);
    STAMP(2);
    if (type == 2) {
#pragma unroll
      for (int u = 0; u < 4; ++u) {
        int head = (ct & 3) * 4 + u;
#pragma unroll
        for (int s = 0; s < 2; ++s) {
          u32x4 v = pack8(acc[u], s);
          if (r >= 28) { unsigned cst = (r == 28) ? 0x3F803F80u : 0u; v[0] = cst; v[1] = cst; v[2] = cst; v[3] = cst; }
          st16(wsr, v, lane * 16, WS_V + ((head * 64 + strip) * 2 + s) * 1024);
        }
      }
    } else if (type < 2) {
      if (ct == 4) {
        float glog = acc[3][12];
        float g = __builtin_amdgcn_rcpf(1.f + __builtin_amdgcn_exp2f(-1.4426950408889634f * glog));
        if (h == 1) st4(wsr, __float_as_uint(g), r * 4, WS_G + strip * 128);
      }
      const float sc = (type == 0) ? (0.1889822365046136f * 1.4426950408889634f) : 1.f;
      const unsigned dst = (type == 0 ? WS_Q : WS_K);
#pragma unroll
      for (int u = 0; u < 4; ++u) {
        int head = (ct & 3) * 4 + u;
#pragma unroll
        for (int k = 0; k < 16; ++k) acc[u][k] *= sc;
#pragma unroll
        for (int s = 0; s < 2; ++s) {
          u32x4 v = pack8(acc[u], s);
          if (s == 1 && h == 1) { v[2] = 0u; v[3] = 0u; }
          st16(wsr, v, lane * 16, dst + ((head * 64 + strip) * 2 + s) * 1024);
        }
      }
    } else {
      LAS float* csl = (LAS float*)(lds + L_P1S);
      LAS float* sts = (LAS float*)(lds + L_P1S + 512);
      const float sx = swap32_add(h ? 0.f : acc[3][8]);
      lds_wait(lds, L_MISC + 16, 2);
      const float sxx = sts[w4 * 32 + r];
      const float mean = sx * (1.f / 448.f);
      const float var = fmaxf(sxx * (1.f / 448.f) - mean * mean, 0.f);
      const float rstd = __builtin_amdgcn_rsqf(var + 1e-5f);
      u32x4 bm = {0u, 0u, 0u, 0u};
      {
        __bf16 mh = (__bf16)(-mean);
        float ml = -mean - (float)mh;
        bm[0] = h ? 0u : pk2((float)mh, (float)mh);
        bm[1] = h ? 0u : pk2(ml, 0.f);
      }
#pragma unroll
      for (int u = 0; u < 4; ++u) {
        float c = csl[u * 32 + r];
        __bf16 chh = (__bf16)c;
        float cl = c - (float)chh;
        u32x4 am = {0u, 0u, 0u, 0u};
        am[0] = pk2((float)chh, cl);
        am[1] = pk2((float)chh, 0.f);
        acc[u] = MFMA32(as_bf8(am), as_bf8(bm), acc[u]);
      }
      const int tok = strip * 32 + r - shift;
      const float rs = (tok >= 0 && tok < S_) ? rstd : 0.f;
      const int g = ct & 3;
      const unsigned lo = ((2 * strip + (r >> 4)) * 14) * 1024 + ((r & 15) + 16 * h) * 16;
#pragma unroll
      for (int f = 0; f < 7; ++f) {
        const int u = f >> 1, s = f & 1;
        u32x4 v;
#pragma unroll
        for (int j = 0; j < 4; ++j) v[j] = gelu2(f32x2{acc[u][8 * s + 2 * j], acc[u][8 * s + 2 * j + 1]}, rs, rs);
        const unsigned so = (u < 3) ? (unsigned)((3 * g + u) * 1024 + s * 512) : (unsigned)((12 + (g >> 1)) * 1024 + (g & 1) * 512);
        st16(wsr, v, lo, WS_H1 + so);
      }
    }
    if (type != 3) lds_arrive(lds, L_MISC + 12, lane);
    STAMP(3);
    WAIT_VM(0);
    tag = read_tag(lds);
    set_flag(ws, fl_p1(rt, ct, wave), tag, lane);
    __builtin_amdgcn_s_setprio(0);
    STAMP(4);
  }

  u32x4 wa[28];
  if (wave < 4) {
    if (hasB) {
      const int s16 = 4 * mt + wave;
      u32x2 xr[4];
      {
        const u16* xp = P.x + (size_t)(16 * s16 + t16) * D_ + 64 * nt + 4 * q4;
#pragma unroll
        for (int tau = 0; tau < 4; ++tau) xr[tau] = *(const u32x2*)(xp + 16 * tau);
      }
      poll_flags(ws, lane < 4 ? fl_p1(mt >> 1, 12 + lane, (s16 >> 1) & 3) : -1, tag);
      __builtin_amdgcn_s_setprio(2);
      STAMP(5);
      u32x4 hf[14];
#pragma unroll
      for (int s = 0; s < 14; ++s) hf[s] = ld16(wsr, lane * 16, WS_H1 + (s16 * 14 + s) * 1024);
      lds_wait(lds, L_MISC + 0, 4);
      const int z = 2 * (q4 & 1) + (q >> 1);
      unsigned a0[4], a1[4];
#pragma unroll
      for (int tau = 0; tau < 4; ++tau) {
        a0[tau] = lds0 + L_R2 + (16 * (q4 >> 1) + 4 * (q4 & 1) + q) * 128 + ((tau ^ z) * 32) + 8 * pp;
        a1[tau] = lds0 + L_R2 + (96 + 112 * (q4 >> 1) + 4 * (q4 & 1) + q) * 128 + ((tau ^ z) * 32) + 8 * pp;
      }
      f32x4 acc[4];
      bf16x8 wf[4];
#pragma unroll
      for (int tau = 0; tau < 4; ++tau) { acc[tau] = f32x4{0.f, 0.f, 0.f, 0.f}; wf[tau] = trf(a0[tau], 1024); }
#pragma unroll
      for (int s = 0; s < 14; ++s) {
#pragma unroll
        for (int tau = 0; tau < 4; ++tau) {
          acc[tau] = MFMA16(wf[tau], as_bf8(hf[s]), acc[tau]);
          const int n = s + 1;
          if (n < 12) wf[tau] = trf(a0[tau] + (112 * (n / 3) + 32 * (n % 3)) * 128, 1024);
          else if (n < 14) wf[tau] = trf(a1[tau] + (224 * (n - 12)) * 128, 1024);
          SB0;
        }
      }
      STAMP(6);
      unsigned v[8];
      float sx = 0.f, sxx = 0.f;
#pragma unroll
      for (int tau = 0; tau < 4; ++tau)
#pragma unroll
        for (int k = 0; k < 2; ++k) {
          unsigned w = pk2(acc[tau][2 * k] + bflo(xr[tau][k]), acc[tau][2 * k + 1] + bfhi(xr[tau][k]));
          v[2 * tau + k] = w;
          sx = dot2(w, 0x3F803F80u, sx);
          sxx = dot2(w, w, sxx);
        }
      st16(wsr, u32x4{v[0], v[1], v[2], v[3]}, lane * 16, WS_X1 + (s16 * 14 + 2 * nt) * 1024);
      st16(wsr, u32x4{v[4], v[5], v[6], v[7]}, lane * 16, WS_X1 + (s16 * 14 + 2 * nt + 1) * 1024);
      sx = swap16_add(swap32_add(sx)); sxx = swap16_add(swap32_add(sxx));
      if (q4 == 0) st8(wsr, u32x2{__float_as_uint(sx), __float_as_uint(sxx)}, t16 * 8, WS_ST + (s16 * 8 + nt) * 128);
      WAIT_VM(0);
      set_flag(ws, FL_X1 + (mt * 7 + nt) * 4 + wave, tag, lane);
      __builtin_amdgcn_s_setprio(0);
      STAMP(7);
    }
    lds_wait(lds, L_MISC + 4, 4);
    {
      const unsigned ab = lds0 + L_W3 + (4 * q4 + q) * 224 + 8 * pp + wave * 32;
      float c0 = 0.f, c1 = 0.f, c2 = 0.f, c3 = 0.f;
#pragma unroll
      for (int s = 0; s < 14; ++s) {
        u32x4 a = __builtin_bit_cast(u32x4, trf(ab + s * 7168, 3584));
        u32x4 bb = __builtin_bit_cast(u32x4, trf(ab + s * 7168 + 128, 3584));
        c0 = dot2(a[0], 0x3F803F80u, c0); c1 = dot2(a[1], 0x3F803F80u, c1); c2 = dot2(bb[0], 0x3F803F80u, c2); c3 = dot2(bb[1], 0x3F803F80u, c3);
        c0 = dot2(a[2], 0x3F803F80u, c0); c1 = dot2(a[3], 0x3F803F80u, c1); c2 = dot2(bb[2], 0x3F803F80u, c2); c3 = dot2(bb[3], 0x3F803F80u, c3);
      }
      c0 = swap16_add(swap32_add(c0 + c1)); c2 = swap16_add(swap32_add(c2 + c3));
      if (q4 < 2) *(LAS float*)(lds + L_CS3 + ((wave + 4 * q4) * 16 + t16) * 4) = q4 ? c2 : c0;
      lds_arrive(lds, L_MISC + 24, lane);
    }
    STAMP(8);
  } else {
    const int head = 2 * xcd + (idx & 1), qs = (idx >> 1) * 4 + (wave - 4);
    {
      int fi = -1;
      const int hw = 0;
      if (lane < 18) {
        int ks = qs - 8 + (lane < 9 ? lane : lane - 9);
        if (ks >= 0) fi = fl_p1(ks >> 2, (lane < 9 ? 4 : 8) + (head >> 2), (ks & 3) + hw);
      } else if (lane == 18) fi = fl_p1(qs >> 2, head >> 2, (qs & 3) + hw);
      else if (lane == 19) fi = fl_p1(qs >> 2, 4, qs & 3);
      poll_flags(ws, fi, tag);
    }
    lds_arrive(lds, L_MISC + 4, lane);
    STAMP(5);
    u32x4 qf0 = ld16(wsr, lane * 16, WS_Q + ((head * 64 + qs) * 2) * 1024);
    u32x4 qf1 = ld16(wsr, lane * 16, WS_Q + ((head * 64 + qs) * 2 + 1) * 1024);
    u32x4 kf[9][2], vf[9][2];
#pragma unroll
    for (int t = 0; t < 9; ++t) {
      int ks = max(qs - 8 + t, 0);
      kf[t][0] = ld16(wsr, lane * 16, WS_K + ((head * 64 + ks) * 2) * 1024);
      kf[t][1] = ld16(wsr, lane * 16, WS_K + ((head * 64 + ks) * 2 + 1) * 1024);
    }
#pragma unroll
    for (int t = 0; t < 9; ++t) {
      int ks = max(qs - 8 + t, 0);
      vf[t][0] = ld16(wsr, lane * 16, WS_V + ((head * 64 + ks) * 2) * 1024);
      vf[t][1] = ld16(wsr, lane * 16, WS_V + ((head * 64 + ks) * 2 + 1) * 1024);
    }
    const float gq = __uint_as_float(ld4(wsr, r * 4, WS_G + qs * 128));
    const float NEG = -1e30f;
    float m = NEG;
#pragma unroll
    for (int t = 0; t < 9; ++t) {
      f32x16 s = MFMA32(as_bf8(kf[t][0]), as_bf8(qf0), splat16(0.f));
      s = MFMA32(as_bf8(kf[t][1]), as_bf8(qf1), s);
      float mt_ = NEG;
#pragma unroll
      for (int i = 0; i < 16; ++i) {
        const int key = (i & 3) + 8 * (i >> 2) + 4 * h;
        float v = s[i];
        if (t == 0) v = (key > r) ? v : NEG;
        if (t == 8) v = (key <= r) ? v : NEG;
        mt_ = fmaxf(mt_, v);
      }
      if (t < 8) mt_ = (qs - 8 + t >= 0) ? mt_ : NEG;
      m = fmaxf(m, mt_);
    }
    m = swap32_max(m);
    const f32x16 negm = splat16(-m);
    f32x16 o = splat16(0.f);
#pragma unroll
    for (int t = 0; t < 9; ++t) {
      if (t == 8 || qs - 8 + t >= 0) {
        f32x16 s = MFMA32(as_bf8(kf[t][0]), as_bf8(qf0), negm);
        s = MFMA32(as_bf8(kf[t][1]), as_bf8(qf1), s);
#pragma unroll
        for (int i = 0; i < 16; ++i) {
          const int key = (i & 3) + 8 * (i >> 2) + 4 * h;
          float v = __builtin_amdgcn_exp2f(s[i]);
          if (t == 0) v = (key > r) ? v : 0.f;
          if (t == 8) v = (key <= r) ? v : 0.f;
          s[i] = v;
        }
        o = MFMA32(as_bf8(vf[t][0]), as_bf8(pack8(s, 0)), o);
        o = MFMA32(as_bf8(vf[t][1]), as_bf8(pack8(s, 1)), o);
      }
    }
    float l = (h == 1) ? o[12] : 0.f;
    l = swap32_add(l);
    const float f = gq * __builtin_amdgcn_rcpf(l);
#pragma unroll
    for (int i = 0; i < 16; ++i) o[i] *= f;
    u32x4 o0 = pack8(o, 0), o1 = pack8(o, 1);
    if (h == 1) { o1[2] = 0u; o1[3] = 0u; }
    st16(wsr, o0, lane * 16, WS_O + ((qs * 16 + head) * 2) * 1024);
    st16(wsr, o1, lane * 16, WS_O + ((qs * 16 + head) * 2 + 1) * 1024);
    WAIT_VM(0);
    set_flag(ws, FL_O + qs * 16 + head, tag, lane);
    STAMP(7);
    lds_wait(lds, L_MISC + 4, 4);
    STAMP(8);
  }

  {
    const int s16 = rt * 8 + wave;
    {
      int fi = -1;
      if (lane < 7) fi = FL_X1 + ((s16 >> 2) * 7 + lane) * 4 + (s16 & 3);
      else if (lane == 7) fi = fl_p1(rt, 4, wave >> 1);
      else if (lane < 10) fi = FL_SV + nt * 16 + 2 * wave + (lane - 8);
      poll_flags(ws, fi, tag);
    }
    STAMP(9);
    u32x4 xf[14];
#pragma unroll
    for (int s = 0; s < 14; ++s) xf[s] = ld16(wsr, lane * 16, WS_X1 + (s16 * 14 + s) * 1024);
    u32x2 st0 = ld8(wsr, q4 * 128 + t16 * 8, WS_ST + s16 * 1024);
    u32x2 st1 = ld8(wsr, min(q4 + 4, 6) * 128 + t16 * 8, WS_ST + s16 * 1024);
    const float g = __uint_as_float(ld4(wsr, t16 * 4, WS_G + s16 * 64));
    const unsigned ab = lds0 + L_W3 + (4 * q4 + q) * 224 + 8 * pp;
    f32x4 acc[7];
    bf16x8 wf[7];
#pragma unroll
    for (int tau = 0; tau < 7; ++tau) { acc[tau] = f32x4{0.f, 0.f, 0.f, 0.f}; wf[tau] = trf(ab + tau * 32, 3584); }
#pragma unroll
    for (int s = 0; s < 14; ++s) {
#pragma unroll
      for (int tau = 0; tau < 7; ++tau) {
        acc[tau] = MFMA16(wf[tau], as_bf8(xf[s]), acc[tau]);
        if (s < 13) wf[tau] = trf(ab + (s + 1) * 7168 + tau * 32, 3584);
        if (tau == 2 || tau == 5) wa[2 * s + (tau == 5)] = ld16(wsr, lane * 16, WS_W4T + ((nt * 8 + wave) * 28 + 2 * s + (tau == 5)) * 1024);
        SB0;
      }
    }
    lds_arrive(lds, L_MISC + 28, lane);
    STAMP(10);
    float sx = __uint_as_float(st0[0]) + (q4 < 3 ? __uint_as_float(st1[0]) : 0.f);
    float sxx = __uint_as_float(st0[1]) + (q4 < 3 ? __uint_as_float(st1[1]) : 0.f);
    sx = swap16_add(swap32_add(sx));
    sxx = swap16_add(swap32_add(sxx));
    const float mean = sx * (1.f / 448.f);
    const float var = fmaxf(sxx * (1.f / 448.f) - mean * mean, 0.f);
    const float rstd = __builtin_amdgcn_rsqf(var + 1e-5f);
    u32x4 bm = {0u, 0u, 0u, 0u};
    {
      __bf16 mh = (__bf16)(-mean);
      float ml = -mean - (float)mh;
      bm[0] = q4 ? 0u : pk2((float)mh, (float)mh);
      bm[1] = q4 ? 0u : pk2(ml, 0.f);
    }
    lds_wait(lds, L_MISC + 24, 4);
#pragma unroll
    for (int tau = 0; tau < 7; ++tau) {
      float c = *(const LAS float*)(lds + L_CS3 + (tau * 16 + t16) * 4);
      __bf16 chh = (__bf16)c;
      float cl = c - (float)chh;
      u32x4 am = {0u, 0u, 0u, 0u};
      am[0] = pk2((float)chh, cl);
      am[1] = pk2((float)chh, 0.f);
      acc[tau] = MFMA16(as_bf8(am), as_bf8(bm), acc[tau]);
    }
    const float rg = rstd * (1.f - g);
    unsigned yy[7][2];
#pragma unroll
    for (int tau = 0; tau < 7; ++tau) {
      yy[tau][0] = gelu2(f32x2{acc[tau][0], acc[tau][1]}, rstd, rg);
      yy[tau][1] = gelu2(f32x2{acc[tau][2], acc[tau][3]}, rstd, rg);
    }
    const unsigned dst = WS_H2 + (((s16 >> 1) * 16 + ct) * 7) * 1024;
    const int tr = 16 * (s16 & 1) + t16;
#pragma unroll
    for (int i = 0; i < 3; ++i) {
      u32x4 v = {yy[2 * i][0], yy[2 * i][1], yy[2 * i + 1][0], yy[2 * i + 1][1]};
      st16(wsr, v, (q4 >> 1) * 1024 + ((q4 & 1) * 32 + tr) * 16, dst + 2 * i * 1024);
    }
    {
      auto r0 = __builtin_amdgcn_permlane16_swap(yy[6][0], yy[6][0], false, false);
      auto r1 = __builtin_amdgcn_permlane16_swap(yy[6][1], yy[6][1], false, false);
      u32x4 v = {yy[6][0], yy[6][1], r0[1], r1[1]};
      if (!(q4 & 1)) st16(wsr, v, ((q4 >> 1) * 32 + tr) * 16, dst + 6 * 1024);
    }
    STAMP(11);
    WAIT_VM(0);
    set_flag(ws, FL_H2 + (rt * 16 + ct) * 8 + wave, tag, lane);
    STAMP(12);
  }

  if (hasB) {
    f32x16 acc[2][2];
#pragma unroll
    for (int i = 0; i < 2; ++i)
#pragma unroll
      for (int t = 0; t < 2; ++t) acc[i][t] = splat16(0.f);
    const int ts = wave >> 2, a_own = wave & 3;
    u32x2 x1v[2];
    float og;
    {
      poll_flags(ws, lane < 4 ? (int)(FL_O + (2 * mt + (lane & 1)) * 16 + 2 * wave + (lane >> 1)) : (lane == 4 ? (int)(FL_SV + 112 + nt * 4 + (wave >> 1)) : -1), tag);
      u32x4 of[2][4], wo_[8];
#pragma unroll
      for (int ks = 0; ks < 4; ++ks) {
#pragma unroll
        for (int i = 0; i < 2; ++i) wo_[2 * ks + i] = ld16(wsr, lane * 16, WS_WOT + ((nt * 8 + wave) * 8 + 2 * ks + i) * 1024);
#pragma unroll
        for (int t = 0; t < 2; ++t) of[t][ks] = ld16(wsr, lane * 16, WS_O + (((2 * mt + t) * 16 + 2 * wave) * 2 + ks) * 1024);
      }
#pragma unroll
      for (int i = 0; i < 2; ++i)
        x1v[i] = ld8(wsr, ((r & 15) + 16 * (2 * (a_own & 1) + h)) * 16 + (a_own >> 1) * 8 + (r >> 4) * 14 * 1024,
                     WS_X1 + ((4 * mt + 2 * ts) * 14 + 2 * nt + i) * 1024);
      og = 1.f - __uint_as_float(ld4(wsr, r * 4, WS_G + (2 * mt + ts) * 128));
#pragma unroll
      for (int ks = 0; ks < 4; ++ks)
#pragma unroll
        for (int t = 0; t < 2; ++t) {
          acc[0][t] = MFMA32(as_bf8(wo_[2 * ks]), as_bf8(of[t][ks]), acc[0][t]);
          acc[1][t] = MFMA32(as_bf8(wo_[2 * ks + 1]), as_bf8(of[t][ks]), acc[1][t]);
        }
    }
    STAMP(14);
    poll_flags(ws, lane < 8 ? (int)(FL_H2 + ((mt >> 1) * 16 + 2 * wave + (lane & 1)) * 8 + 4 * (mt & 1) + (lane >> 1)) : -1, tag);
    STAMP(15);
    constexpr int WIN = 11;
    u32x4 hf[28];
    const unsigned h2base = WS_H2 + (((2 * mt) * 16 + 2 * wave) * 7) * 1024;
#pragma unroll
    for (int i = 0; i < WIN; ++i) hf[i] = ld16(wsr, lane * 16, h2base + (i / 14) * 112 * 1024 + (i % 14) * 1024);
#pragma unroll
    for (int t = 0; t < 2; ++t) {
#pragma unroll
      for (int ks = 0; ks < 14; ++ks) {
        acc[0][t] = MFMA32(as_bf8(wa[2 * ks]), as_bf8(hf[14 * t + ks]), acc[0][t]);
        acc[1][t] = MFMA32(as_bf8(wa[2 * ks + 1]), as_bf8(hf[14 * t + ks]), acc[1][t]);
        const int i = 14 * t + ks + WIN;
        if (i < 28) {
          hf[i] = ld16(wsr, lane * 16, h2base + (i / 14) * 112 * 1024 + (i % 14) * 1024);
          SB0;
        }
      }
      if (t == 0) lds_wait(lds, L_MISC + 28, 8);
#pragma unroll
      for (int a = 0; a < 4; ++a)
#pragma unroll
        for (int i = 0; i < 2; ++i) {
          const f32x16& A = acc[i][t];
          const int p = 8 * t + 2 * a + i;
          const unsigned ad = lds0 + wave * 16384 + p * 1024;
          *(LAS f32x4*)(size_t)(ad + lane * 16) = f32x4{A[4 * a], A[4 * a + 1], A[4 * a + 2], A[4 * a + 3]};
        }
    }
    STAMP(16);
    BARRIER();
    f32x4 fin[2];
    {
      const unsigned rb = lds0 + 2 * wave * 1024 + lane * 16;
      const unsigned rs = 16384u;
#pragma unroll
      for (int i = 0; i < 2; ++i) {
        f32x4 v[8];
#pragma unroll
        for (int si = 0; si < 8; ++si) v[si] = *(const LAS f32x4*)(size_t)(rb + si * rs + i * 1024);
        fin[i] = ((v[0] + v[1]) + (v[2] + v[3])) + ((v[4] + v[5]) + (v[6] + v[7]));
      }
    }
    unsigned d[2][2];
#pragma unroll
    for (int i = 0; i < 2; ++i)
#pragma unroll
      for (int k = 0; k < 2; ++k)
        d[i][k] = pk2(__builtin_fmaf(og, bflo(x1v[i][k]), fin[i][2 * k]), __builtin_fmaf(og, bfhi(x1v[i][k]), fin[i][2 * k + 1]));
    auto s0 = __builtin_amdgcn_permlane32_swap(d[0][0], d[1][0], false, false);
    auto s1 = __builtin_amdgcn_permlane32_swap(d[0][1], d[1][1], false, false);
    u32x4 o = {s0[0], s1[0], s0[1], s1[1]};
    *(u32x4*)(P.out + (size_t)(64 * mt + 32 * ts + r) * D_ + 64 * nt + 32 * h + 8 * a_own) = o;
    STAMP(17);
  } else if (b == 0 && wave == 0) {
#pragma unroll 1
    for (int i = 0; i < 4; ++i) poll_flags(ws, FL_P1 + (i * 64 + lane) * 8, tag);
    if (lane == 0) __hip_atomic_store((GAS u64*)(ws + WS_EPOCH), tag, RLX_AGENT);
  }
}

extern "C" void kernel_launch(void* const* d_in, const int* in_sizes, int n_in, void* d_out, int out_size, void* d_ws,
                              size_t ws_size, hipStream_t stream) {
  Params P;
  P.x = (const u16*)d_in[0];
  P.wqkv = (const u16*)d_in[1];
  P.wo = (const u16*)d_in[2];
  P.w1 = (const u16*)d_in[4];
  P.w2 = (const u16*)d_in[5];
  P.w3 = (const u16*)d_in[6];
  P.w4 = (const u16*)d_in[7];
  P.gw = (const u16*)d_in[8];
  P.ws = (char*)d_ws;
  P.out = (u16*)d_out;
  (void)hipFuncSetAttribute((const void*)mega_kernel, hipFuncAttributeMaxDynamicSharedMemorySize, LDS_BYTES);
  mega_kernel<<<256, 512, LDS_BYTES, stream>>>(P);
}
```
